```python
import torch

def _softmax(x, axis=-1):
    return torch.softmax(x, dim=axis)
def _gelu(x):
    return torch.nn.functional.gelu(x, approximate="tanh")
def _silu(x):
    return torch.nn.functional.silu(x)
def _sigmoid(x):
    return torch.sigmoid(x)
def _layernorm(x, axis=-1, eps=1e-5):
    m = x.mean(dim=axis, keepdim=True)
    v = x.var(dim=axis, keepdim=True, unbiased=False)
    return (x - m) / torch.sqrt(v + eps)
def _scatter_add(tgt, idx, src, axis=0):
    return tgt.clone().index_add_(axis, idx, src)
def _segment_sum(src, seg, n):
    out = torch.zeros((n,) + src.shape[1:], dtype=src.dtype)
    return out.index_add_(0, seg, src)
def _pad(x, pw):
    flat = tuple(v for pair in reversed(pw) for v in pair)
    return torch.nn.functional.pad(x, flat)

kernel_name = 'gated_mix__mha_block__dense_activation_relu__prompt'

def setup_inputs():

    _w = (lambda a: (lambda t: t.to(torch.bfloat16) if t.is_floating_point() else t)(torch.as_tensor(a)))
    def _A_setup():
        B, S, D, H = 1, 2048, 640, 32
        return {
            "x": _w((lambda *s: torch.randn(*s, dtype=torch.bfloat16) * 0.05)(B, S, D)),
            "wqkv": _w((lambda *s: torch.randn(*s, dtype=torch.bfloat16) * 0.8)(D, 3 * D)),
            "wo": _w((lambda *s: torch.randn(*s, dtype=torch.bfloat16) * 0.2)(D, D)),
        }
    _A_in = _A_setup()

    def _B_setup():
        B, S, D, Dff = 1, 2048, 640, 1728
        return {
            "x": _w((lambda *s: torch.randn(*s, dtype=torch.bfloat16) * 0.05)(B, S, D)),
            "w1": _w((lambda *s: torch.randn(*s, dtype=torch.bfloat16) * 0.05)(D, Dff)),
            "w2": _w((lambda *s: torch.randn(*s, dtype=torch.bfloat16) * 0.05)(Dff, D)),
        }
    _B_in = _B_setup()

    return {
        "A_x": _A_in["x"],
        "A_wqkv": _A_in["wqkv"],
        "A_wo": _A_in["wo"],
        "B_x": _B_in["x"],
        "B_w1": _B_in["w1"],
        "B_w2": _B_in["w2"],
        "gate_w": _w((lambda *s: torch.randn(*s, dtype=torch.bfloat16) * 0.05)(640) * 16.0),
    }

def reference(A_x, A_wqkv, A_wo, B_x, B_w1, B_w2, gate_w):
    def _A_ref(x, wqkv, wo):
        B, S, D = x.shape
        H = 32
        Dh = D // H
        qkv = (lambda x, *d: x.permute(*d))((x @ wqkv).reshape(B, S, 3, H, Dh), 2, 0, 3, 1, 4)
        q, k, v = qkv[0], qkv[1], qkv[2]
        scores = torch.einsum("bhqd,bhkd->bhqk", q, k) * (Dh ** -0.5)
        p = _softmax(scores, axis=-1)
        o = torch.einsum("bhqk,bhkd->bhqd", p, v)
        return (lambda x, *d: x.permute(*d))(o, 0, 2, 1, 3).reshape(B, S, D) @ wo

    def _B_ref(x, w1, w2):
        h = torch.einsum("bsd,df->bsf", x, w1)
        zero = torch.zeros_like(h)
        h = torch.maximum(h, zero)
        return torch.einsum("bsf,fd->bsd", h, w2)

    x = A_x
    g = _sigmoid((x * gate_w).sum(-1, keepdims=True))
    a = _A_ref(x, A_wqkv, A_wo)
    b = _B_ref(x, B_w1, B_w2)
    return g * a + (1 - g) * b
```

```cpp
#include <hip/hip_runtime.h>
#include <hip/hip_bf16.h>
#include <stdint.h>

typedef __attribute__((ext_vector_type(8))) short bf16x8;
typedef __attribute__((ext_vector_type(4))) short bf16x4;
typedef __attribute__((ext_vector_type(16))) float f32x16;
typedef __attribute__((ext_vector_type(4))) float f32x4;
typedef __attribute__((ext_vector_type(2))) float f32x2;
typedef __attribute__((ext_vector_type(4))) unsigned u32x4;
typedef __attribute__((ext_vector_type(2))) unsigned u32x2;
typedef __attribute__((ext_vector_type(2))) __bf16 bf16x2_t;
typedef unsigned short u16;
#define LDSP __attribute__((address_space(3)))
#define DI __device__ __forceinline__

constexpr int SEQ = 2048, DM = 640, NH = 32, DFF = 1728;
constexpr int KV_STAGE = 20992;
constexpr int KV_VOFF = 10240;
constexpr size_t QF_OFF = 0;
constexpr size_t KV_OFF = QF_OFF + (size_t)32 * 64 * 1280;
constexpr size_t HF_OFF = KV_OFF + (size_t)32 * 8 * KV_STAGE;
constexpr size_t W2F_OFF = HF_OFF + (size_t)64 * 108 * 1024;
constexpr size_t WOF_OFF = W2F_OFF + (size_t)108 * 20 * 1024;
constexpr size_t OF_OFF = WOF_OFF + (size_t)40 * 20 * 1024;
constexpr size_t B_OFF = OF_OFF + (size_t)64 * 40 * 1024;
constexpr size_t G_OFF = B_OFF + (size_t)2048 * 640 * 2;

#define MFMA32(a, b, c) __builtin_amdgcn_mfma_f32_32x32x16_bf16((a), (b), (c), 0, 0, 0)
#define MFMA32K8(a, b, c) __builtin_amdgcn_mfma_f32_32x32x8bf16_1k((a), (b), (c), 0, 0, 0)

DI unsigned pk2(float lo, float hi) {
  f32x2 v = {lo, hi};
  bf16x2_t r = __builtin_convertvector(v, bf16x2_t);
  return __builtin_bit_cast(unsigned, r);
}
DI float bf_lo(unsigned u) { return __uint_as_float(u << 16); }
DI float bf_hi(unsigned u) { return __uint_as_float(u & 0xffff0000u); }

DI void glds16(const void* g, unsigned lds_dst) {
  unsigned sv;
  asm volatile("s_mov_b32 %0, m0\n\ts_mov_b32 m0, %2\n\ts_nop 0\n\tglobal_load_lds_dwordx4 %1, off\n\ts_mov_b32 m0, %0"
               : "=&s"(sv) : "v"(g), "s"(lds_dst) : "memory");
}
DI bf16x4 tr_read(const LDSP char* p) {
  return __builtin_amdgcn_ds_read_tr16_b64_v4i16((LDSP bf16x4*)p);
}
#define WAITV(n) asm volatile("s_waitcnt vmcnt(" #n ")" ::: "memory")
#define BARRIER() asm volatile("s_waitcnt lgkmcnt(0)\n\ts_barrier" ::: "memory")

constexpr int K1_SLOT = 49152;

template <bool TR>
DI void k1_mainloop(f32x16 (&acc)[2][2], LDSP char* lds, const u16* (&asrc)[4], const u16* (&bsrc)[2], int ldw,
                    unsigned lds_a_dst, unsigned lds_b_dst, const int (&aoff)[4], const int (&boff)[2]) {
  auto issue = [&](int kt, int slot) {
#pragma unroll
    for (int i = 0; i < 4; ++i) glds16(asrc[i] + kt * 64, lds_a_dst + slot * K1_SLOT + i * 1024);
#pragma unroll
    for (int i = 0; i < 2; ++i) glds16(bsrc[i] + (size_t)kt * 64 * ldw, lds_b_dst + slot * K1_SLOT + i * 1024);
  };
  issue(0, 0);
  issue(1, 1);
#pragma unroll
  for (int kt = 0; kt < 10; ++kt) {
    if (kt < 9) WAITV(6); else WAITV(0);
    BARRIER();
    if (kt + 2 < 10) issue(kt + 2, (kt + 2) % 3);
    const LDSP char* sa = lds + (kt % 3) * K1_SLOT;
    const LDSP char* sb = sa + 32768;
#pragma unroll
    for (int ks = 0; ks < 4; ++ks) {
      bf16x8 a[2], b[2];
#pragma unroll
      for (int mt = 0; mt < 2; ++mt) a[mt] = *(const LDSP bf16x8*)(sa + aoff[ks] + mt * 4096);
#pragma unroll
      for (int nt = 0; nt < 2; ++nt) {
        bf16x4 lo = tr_read(sb + boff[nt] + ks * 4096);
        bf16x4 hi = tr_read(sb + boff[nt] + ks * 4096 + 1024);
        b[nt] = __builtin_shufflevector(lo, hi, 0, 1, 2, 3, 4, 5, 6, 7);
      }
#pragma unroll
      for (int mt = 0; mt < 2; ++mt)
#pragma unroll
        for (int nt = 0; nt < 2; ++nt)
          acc[mt][nt] = TR ? MFMA32(b[nt], a[mt], acc[mt][nt]) : MFMA32(a[mt], b[nt], acc[mt][nt]);
    }
  }
}

DI void k1_aux(int aux, const u16* x, const u16* w2, const u16* wo, const u16* gw, char* ws) {
  const int tid = threadIdx.x;
  for (int u = aux * 512 + tid; u < 23680; u += 24 * 512) {
    const bool isw2 = u < 17280;
    const int v = isw2 ? u : u - 17280;
    const int co = v % 80, t = v / 80, hh = t & 1, ks = t >> 1;
    const u16* src = isw2 ? w2 : wo;
    u32x4 R[8];
#pragma unroll
    for (int j = 0; j < 8; ++j) {
      int row;
      if (isw2) row = 16 * ks + 8 * (j >> 2) + 4 * hh + (j & 3);
      else if (ks < 32) row = 20 * ks + 8 * (j >> 2) + 4 * hh + (j & 3);
      else row = 20 * (4 * (ks - 32) + 2 * hh + (j >> 2)) + 16 + (j & 3);
      R[j] = *(const u32x4*)(src + (size_t)row * 640 + co * 8);
    }
    char* dst = ws + (isw2 ? W2F_OFF : WOF_OFF) + ((size_t)ks * 20 + (co >> 2)) * 1024 + hh * 512 + (co & 3) * 128;
#pragma unroll
    for (int c = 0; c < 8; ++c) {
      u32x4 o;
#pragma unroll
      for (int jj = 0; jj < 4; ++jj)
        o[jj] = __builtin_amdgcn_perm(R[2 * jj + 1][c >> 1], R[2 * jj][c >> 1], (c & 1) ? 0x07060302u : 0x05040100u);
      *(u32x4*)(dst + c * 16) = o;
    }
  }
  const int wv = aux * 8 + (tid >> 6), lane = tid & 63, sub = lane >> 4, l16 = lane & 15;
  float gwf[5][8];
#pragma unroll
  for (int c = 0; c < 5; ++c) {
    u32x4 g4 = *(const u32x4*)(gw + (c * 16 + l16) * 8);
#pragma unroll
    for (int e = 0; e < 4; ++e) { gwf[c][2 * e] = bf_lo(g4[e]); gwf[c][2 * e + 1] = bf_hi(g4[e]); }
  }
  float* gout = (float*)(ws + G_OFF);
  for (int r0 = wv * 4; r0 < SEQ; r0 += 192 * 4) {
    const int row = r0 + sub;
    float s = 0.f;
#pragma unroll
    for (int c = 0; c < 5; ++c) {
      u32x4 x4 = *(const u32x4*)(x + (size_t)row * 640 + (c * 16 + l16) * 8);
#pragma unroll
      for (int e = 0; e < 4; ++e) { s += bf_lo(x4[e]) * gwf[c][2 * e]; s += bf_hi(x4[e]) * gwf[c][2 * e + 1]; }
    }
    s += __shfl_xor(s, 1); s += __shfl_xor(s, 2); s += __shfl_xor(s, 4); s += __shfl_xor(s, 8);
    if (l16 == 0) gout[row] = 1.f / (1.f + __expf(-s));
  }
}

__global__ __launch_bounds__(512) void k1_kernel(const u16* __restrict__ x, const u16* __restrict__ wqkv,
                                                 const u16* __restrict__ w1, const u16* __restrict__ w2,
                                                 const u16* __restrict__ wo, const u16* __restrict__ gw,
                                                 char* __restrict__ ws) {
  __shared__ __attribute__((aligned(16))) char lds_raw[3 * K1_SLOT];
  const int bid = blockIdx.x, xcd = bid & 7, idx = bid >> 3;
  if (idx >= 29) { k1_aux(xcd * 3 + (idx - 29), x, w2, wo, gw, ws); return; }
  const int ii = (xcd & 3) * 29 + idx, nti = ii >> 2, mti = (xcd >> 2) * 4 + (ii & 3);
  const int tid = threadIdx.x, lane = tid & 63, wid = __builtin_amdgcn_readfirstlane(tid >> 6);
  const int r = lane & 31, hh = lane >> 5, wm = wid >> 1, wn = wid & 1;
  const int m0 = mti * 256;
  const bool isq = nti < 15;
  const u16* W = isq ? wqkv : w1;
  const int ldw = isq ? 1920 : DFF;
  const int n0 = isq ? nti * 128 : (nti - 15) * 128;
  const bool half_tile = (nti == 28);
  LDSP char* lds = (LDSP char*)lds_raw;
  const unsigned lds0 = (unsigned)(uintptr_t)lds;

  const u16* asrc[4]; const u16* bsrc[2];
#pragma unroll
  for (int i = 0; i < 4; ++i) {
    const int row = 8 * (wid * 4 + i) + (lane >> 3);
    const int c = (lane & 7) ^ ((row >> 1) & 7);
    asrc[i] = x + (size_t)(m0 + row) * 640 + c * 8;
  }
#pragma unroll
  for (int i = 0; i < 2; ++i) {
    const int row = 4 * (wid * 2 + i) + (lane >> 4);
    int c = (lane & 15) ^ ((row & 3) << 2);
    if (half_tile) c &= 7;
    bsrc[i] = W + (size_t)row * ldw + n0 + c * 8;
  }
  int aoff[4], boff[2];
#pragma unroll
  for (int ks = 0; ks < 4; ++ks) aoff[ks] = (wm * 64 + r) * 128 + (((2 * ks + hh) ^ ((r >> 1) & 7)) * 16);
  {
    const int q = (lane & 15) >> 2, p = lane & 3, blk = (lane >> 4) & 1;
#pragma unroll
    for (int nt = 0; nt < 2; ++nt)
      boff[nt] = (8 * hh + q) * 256 + ((((wn * 2 + nt) ^ q) * 4 + 2 * blk + (p >> 1)) * 16) + 8 * (p & 1);
  }
  f32x16 acc[2][2];
#pragma unroll
  for (int mt = 0; mt < 2; ++mt)
#pragma unroll
    for (int nt = 0; nt < 2; ++nt)
#pragma unroll
      for (int e = 0; e < 16; ++e) acc[mt][nt][e] = 0.f;

  const unsigned lds_a_dst = lds0 + wid * 4096, lds_b_dst = lds0 + 32768 + wid * 2048;
  const bool isv = (nti >= 10 && nti < 15);
  if (isv) {
    k1_mainloop<false>(acc, lds, asrc, bsrc, ldw, lds_a_dst, lds_b_dst, aoff, boff);
#pragma unroll
    for (int nt = 0; nt < 2; ++nt) {
      const int col = (nti - 10) * 128 + wn * 64 + nt * 32 + r;
      const int head = col / 20, d = col - head * 20;
#pragma unroll
      for (int mt = 0; mt < 2; ++mt) {
        const int rowbase = m0 + wm * 64 + mt * 32;
        char* dst = ws + KV_OFF + (size_t)(head * 8 + (rowbase >> 8)) * KV_STAGE + KV_VOFF + ((rowbase & 255) >> 5) * 1344 +
                    hh * 336 + d * 16;
#pragma unroll
        for (int s = 0; s < 2; ++s) {
          u32x4 o;
#pragma unroll
          for (int e = 0; e < 4; ++e) o[e] = pk2(acc[mt][nt][8 * s + 2 * e], acc[mt][nt][8 * s + 2 * e + 1]);
          *(u32x4*)(dst + s * 672) = o;
          if (d == 19) { u32x4 ones = {0x3f803f80u, 0x3f803f80u, 0x3f803f80u, 0x3f803f80u}; *(u32x4*)(dst + s * 672 + 16) = ones; }
        }
      }
    }
  } else {
    k1_mainloop<true>(acc, lds, asrc, bsrc, ldw, lds_a_dst, lds_b_dst, aoff, boff);
    if (nti < 10) {
      const bool q_ = nti < 5;
      const float sc = q_ ? 0.22360679774997896f * 1.4426950408889634f : 1.f;
#pragma unroll
      for (int nt = 0; nt < 2; ++nt) {
        const int c0 = (q_ ? nti : nti - 5) * 128 + wn * 64 + nt * 32;
#pragma unroll
        for (int g = 0; g < 4; ++g) {
          const int G = (c0 >> 2) + 2 * g + hh, head = G / 5, gi = G - head * 5;
          const int inner = gi < 4 ? (gi >> 1) * 512 + r * 16 + (gi & 1) * 8 : 1024 + r * 8;
#pragma unroll
          for (int mt = 0; mt < 2; ++mt) {
            const int rowbase = m0 + wm * 64 + mt * 32;
            char* dst = q_ ? ws + QF_OFF + (size_t)(head * 64 + (rowbase >> 5)) * 1280 + inner
                           : ws + KV_OFF + (size_t)(head * 8 + (rowbase >> 8)) * KV_STAGE + ((rowbase & 255) >> 5) * 1280 + inner;
            u32x2 o;
            o[0] = pk2(acc[mt][nt][4 * g] * sc, acc[mt][nt][4 * g + 1] * sc);
            o[1] = pk2(acc[mt][nt][4 * g + 2] * sc, acc[mt][nt][4 * g + 3] * sc);
            *(u32x2*)dst = o;
          }
        }
      }
    } else if (!(half_tile && wn == 1)) {
#pragma unroll
      for (int nt = 0; nt < 2; ++nt) {
        const int ks0 = ((nti - 15) * 128 + wn * 64 + nt * 32) >> 4;
#pragma unroll
        for (int mt = 0; mt < 2; ++mt) {
          const int rt = (m0 + wm * 64 + mt * 32) >> 5;
#pragma unroll
          for (int s = 0; s < 2; ++s) {
            u32x4 o;
#pragma unroll
            for (int e = 0; e < 4; ++e)
              o[e] = pk2(fmaxf(acc[mt][nt][8 * s + 2 * e], 0.f), fmaxf(acc[mt][nt][8 * s + 2 * e + 1], 0.f));
            *(u32x4*)(ws + HF_OFF + ((size_t)rt * 108 + ks0 + s) * 1024 + lane * 16) = o;
          }
        }
      }
    }
  }
}

constexpr int K2_RING = 3 * KV_STAGE;
constexpr int PART_ROW = 656;
constexpr int PART_SZ = 32 * PART_ROW;
constexpr int K2_LDS = K2_RING + 4 * PART_SZ;

DI void pack_p(const f32x16& s, bf16x8& p0, bf16x8& p1) {
  u32x4 a, b;
#pragma unroll
  for (int e = 0; e < 4; ++e) { a[e] = pk2(s[2 * e], s[2 * e + 1]); b[e] = pk2(s[8 + 2 * e], s[8 + 2 * e + 1]); }
  p0 = __builtin_bit_cast(bf16x8, a); p1 = __builtin_bit_cast(bf16x8, b);
}
DI float xhalf_max(float m) { return fmaxf(m, __shfl_xor(m, 32)); }

DI void attn_fallback(const char* kvh, bf16x8 q0, bf16x4 q1, int lane, f32x16& o_out) {
  const int r = lane & 31, hh = lane >> 5, rv = r < 20 ? r : 20;
  f32x16 o, zero;
#pragma unroll
  for (int e = 0; e < 16; ++e) { o[e] = 0.f; zero[e] = 0.f; }
  float m = -INFINITY;
  for (int t = 0; t < 64; ++t) {
    const char* st = kvh + (size_t)(t >> 3) * KV_STAGE;
    const int tt = t & 7;
    bf16x8 kf0 = *(const bf16x8*)(st + tt * 1280 + lane * 16);
    bf16x4 kf1 = *(const bf16x4*)(st + tt * 1280 + 1024 + r * 8);
    bf16x8 vf0 = *(const bf16x8*)(st + KV_VOFF + tt * 1344 + hh * 336 + rv * 16);
    bf16x8 vf1 = *(const bf16x8*)(st + KV_VOFF + tt * 1344 + 672 + hh * 336 + rv * 16);
    f32x16 s = MFMA32(kf0, q0, zero);
    s = MFMA32K8(kf1, q1, s);
    float tm = s[0];
#pragma unroll
    for (int e = 1; e < 16; ++e) tm = fmaxf(tm, s[e]);
    tm = xhalf_max(tm);
    const float mn = fmaxf(m, tm);
    const float alpha = __builtin_amdgcn_exp2f(m - mn);
    m = mn;
#pragma unroll
    for (int e = 0; e < 16; ++e) { o[e] *= alpha; s[e] = __builtin_amdgcn_exp2f(s[e] - mn); }
    bf16x8 p0, p1;
    pack_p(s, p0, p1);
    o = MFMA32(vf0, p0, o);
    o = MFMA32(vf1, p1, o);
  }
  o_out = o;
}

__global__ __launch_bounds__(768) void k2_kernel(char* __restrict__ ws) {
  __shared__ __attribute__((aligned(16))) char lds_raw[K2_LDS];
  LDSP char* lds = (LDSP char*)lds_raw;
  const unsigned lds0 = (unsigned)(uintptr_t)lds;
  const int bid = blockIdx.x, xcd = bid & 7, idx = bid >> 3;
  const int tid = threadIdx.x, lane = tid & 63, wid = __builtin_amdgcn_readfirstlane(tid >> 6);
  const int r = lane & 31, hh = lane >> 5;
  if (wid < 8) {
    const int head = xcd * 4 + (idx >> 3), qt = idx & 7;
    const char* kvh = ws + KV_OFF + (size_t)head * 8 * KV_STAGE;
    const char* dsrc = kvh + tid * 16;
    auto dma = [&](int stage, int slot) {
      const char* s = dsrc + (size_t)stage * KV_STAGE;
      const unsigned d = lds0 + slot * KV_STAGE + wid * 1024;
      glds16(s, d);
      glds16(s + 8192, d + 8192);
      if (tid < 288) glds16(s + 16384, d + 16384);
    };
    dma(0, 0);
    dma(1, 1);
    const int rt = qt * 8 + wid;
    const char* qsrc = ws + QF_OFF + (size_t)(head * 64 + rt) * 1280;
    const bf16x8 q0 = *(const bf16x8*)(qsrc + lane * 16);
    bf16x4 q1 = *(const bf16x4*)(qsrc + 1024 + r * 8);
    if (hh) q1 = bf16x4{0, 0, 0, 0};
    const int rv = r < 20 ? r : 20;
    const int koff0 = lane * 16, koff1 = 1024 + r * 8, voff = KV_VOFF + hh * 336 + rv * 16;
    f32x16 o, negm;
#pragma unroll
    for (int e = 0; e < 16; ++e) { o[e] = 0.f; negm[e] = 0.f; }
    WAITV(0);
    BARRIER();
    bf16x8 kf0 = *(const LDSP bf16x8*)(lds + koff0);
    bf16x4 kf1 = *(const LDSP bf16x4*)(lds + koff1);
    {
      f32x16 s = MFMA32(kf0, q0, negm);
      s = MFMA32K8(kf1, q1, s);
      float tm = s[0];
#pragma unroll
      for (int e = 1; e < 16; ++e) tm = fmaxf(tm, s[e]);
      tm = xhalf_max(tm);
#pragma unroll
      for (int e = 0; e < 16; ++e) negm[e] = -tm;
    }
    f32x16 s_cur = MFMA32(kf0, q0, negm);
    s_cur = MFMA32K8(kf1, q1, s_cur);
    kf0 = *(const LDSP bf16x8*)(lds + 1280 + koff0);
    kf1 = *(const LDSP bf16x4*)(lds + 1280 + koff1);
    int slot = 0;
    for (int stage = 0; stage < 8; ++stage) {
      const int nslot = slot == 2 ? 0 : slot + 1;
      const int pslot = nslot == 2 ? 0 : nslot + 1;
      if (stage + 2 < 8) dma(stage + 2, pslot);
      const LDSP char* cb = lds + slot * KV_STAGE;
      const LDSP char* nb = lds + nslot * KV_STAGE;
#pragma unroll
      for (int t = 0; t < 8; ++t) {
        bf16x8 vf0 = *(const LDSP bf16x8*)(cb + voff + t * 1344);
        bf16x8 vf1 = *(const LDSP bf16x8*)(cb + voff + t * 1344 + 672);
        f32x16 s_nxt = MFMA32(kf0, q0, negm);
        s_nxt = MFMA32K8(kf1, q1, s_nxt);
        {
          const LDSP char* kb = (t + 2 < 8) ? cb + (t + 2) * 1280 : nb + (t + 2 - 8) * 1280;
          kf0 = *(const LDSP bf16x8*)(kb + koff0);
          kf1 = *(const LDSP bf16x4*)(kb + koff1);
        }
#pragma unroll
        for (int e = 0; e < 16; ++e) s_cur[e] = __builtin_amdgcn_exp2f(s_cur[e]);
        bf16x8 p0, p1;
        pack_p(s_cur, p0, p1);
        o = MFMA32(vf0, p0, o);
        o = MFMA32(vf1, p1, o);
        s_cur = s_nxt;
      }
      slot = nslot;
      if (stage < 7) { WAITV(0); BARRIER(); }
    }
    {
      float l = __shfl(o[8], 32 + r);
      float amax = 0.f;
#pragma unroll
      for (int e = 0; e < 12; ++e) amax = fmaxf(amax, fabsf(o[e]));
      bool bad = !(l <= 1.8446744e19f) || !(amax <= 3.0e38f);
#pragma unroll
      for (int e = 0; e < 12; ++e) bad = bad || (o[e] != o[e]);
      if (__any(bad)) attn_fallback(kvh, q0, q1, lane, o);
    }
    const float l = __shfl(o[8], 32 + r);
    const float inv = 1.f / l;
    char* odst = ws + OF_OFF + (size_t)rt * 40 * 1024;
    u32x4 om;
#pragma unroll
    for (int e = 0; e < 4; ++e) om[e] = pk2(o[2 * e] * inv, o[2 * e + 1] * inv);
    *(u32x4*)(odst + head * 1024 + lane * 16) = om;
    if (hh == 0) {
      u32x2 orr;
      orr[0] = pk2(o[8] * inv, o[9] * inv);
      orr[1] = pk2(o[10] * inv, o[11] * inv);
      *(u32x2*)(odst + (32 + (head >> 2)) * 1024 + (((head >> 1) & 1) * 32 + r) * 16 + (head & 1) * 8) = orr;
    }
  } else {
    const int kq = wid - 8;
    const int rt = xcd * 8 + (idx >> 2), cg = idx & 3;
    const char* asrc = ws + HF_OFF + ((size_t)rt * 108 + kq * 27) * 1024 + lane * 16;
    const char* bsrc = ws + W2F_OFF + ((size_t)kq * 27 * 20 + cg * 5) * 1024 + lane * 16;
    f32x16 acc[5];
#pragma unroll
    for (int c = 0; c < 5; ++c)
#pragma unroll
      for (int e = 0; e < 16; ++e) acc[c][e] = 0.f;
    bf16x8 af[2], bfr[2][5];
#pragma unroll
    for (int j = 0; j < 2; ++j) {
      af[j] = *(const bf16x8*)(asrc + j * 1024);
#pragma unroll
      for (int c = 0; c < 5; ++c) bfr[j][c] = *(const bf16x8*)(bsrc + (size_t)j * 20480 + c * 1024);
    }
    BARRIER();
    for (int stage = 0; stage < 7; ++stage) {
#pragma unroll
      for (int i = 0; i < 4; ++i) {
        const int k = stage * 4 + i;
        if (k < 27) {
          const int j = i & 1;
#pragma unroll
          for (int c = 0; c < 5; ++c) acc[c] = MFMA32(bfr[j][c], af[j], acc[c]);
          if (k + 2 < 27) {
            af[j] = *(const bf16x8*)(asrc + (k + 2) * 1024);
#pragma unroll
            for (int c = 0; c < 5; ++c) bfr[j][c] = *(const bf16x8*)(bsrc + (size_t)(k + 2) * 20480 + c * 1024);
          }
        }
      }
      if (stage == 6) {
        LDSP char* pb = lds + K2_RING + kq * PART_SZ + r * PART_ROW + hh * 16;
#pragma unroll
        for (int c = 0; c < 5; ++c)
#pragma unroll
          for (int g = 0; g < 4; ++g) {
            f32x4 v = {acc[c][4 * g], acc[c][4 * g + 1], acc[c][4 * g + 2], acc[c][4 * g + 3]};
            *(LDSP f32x4*)(pb + c * 128 + g * 32) = v;
          }
      }
      BARRIER();
    }
    for (int it = tid - 512; it < 640; it += 256) {
      const int row = it / 20, c8 = it - row * 20;
      const LDSP char* pb = lds + K2_RING + row * PART_ROW + c8 * 32;
      f32x4 s0 = *(const LDSP f32x4*)(pb), s1 = *(const LDSP f32x4*)(pb + 16);
#pragma unroll
      for (int w = 1; w < 4; ++w) {
        s0 += *(const LDSP f32x4*)(pb + w * PART_SZ);
        s1 += *(const LDSP f32x4*)(pb + w * PART_SZ + 16);
      }
      u32x4 o = {pk2(s0[0], s0[1]), pk2(s0[2], s0[3]), pk2(s1[0], s1[1]), pk2(s1[2], s1[3])};
      *(u32x4*)(ws + B_OFF + ((size_t)(rt * 32 + row) * 640 + cg * 160 + c8 * 8) * 2) = o;
    }
  }
}

template <int NT>
DI void k3_part(const char* asrc, const char* bsrc, LDSP char* pb) {
  bf16x8 af[10], bfr[10][NT];
#pragma unroll
  for (int k = 0; k < 10; ++k) {
    af[k] = *(const bf16x8*)(asrc + k * 1024);
#pragma unroll
    for (int c = 0; c < NT; ++c) bfr[k][c] = *(const bf16x8*)(bsrc + (size_t)k * 20480 + c * 1024);
  }
  f32x16 acc[NT];
#pragma unroll
  for (int c = 0; c < NT; ++c)
#pragma unroll
    for (int e = 0; e < 16; ++e) acc[c][e] = 0.f;
#pragma unroll
  for (int k = 0; k < 10; ++k)
#pragma unroll
    for (int c = 0; c < NT; ++c) acc[c] = MFMA32(bfr[k][c], af[k], acc[c]);
#pragma unroll
  for (int c = 0; c < NT; ++c)
#pragma unroll
    for (int g = 0; g < 4; ++g) {
      f32x4 v = {acc[c][4 * g], acc[c][4 * g + 1], acc[c][4 * g + 2], acc[c][4 * g + 3]};
      *(LDSP f32x4*)(pb + c * 128 + g * 32) = v;
    }
}

__global__ __launch_bounds__(512) void k3_kernel(const char* __restrict__ ws, u16* __restrict__ out) {
  __shared__ __attribute__((aligned(16))) char lds_raw[4 * PART_SZ];
  LDSP char* lds = (LDSP char*)lds_raw;
  const int bid = blockIdx.x, xcd = bid & 7, idx = bid >> 3;
  const int tid = threadIdx.x, lane = tid & 63, wid = __builtin_amdgcn_readfirstlane(tid >> 6);
  const int r = lane & 31, hh = lane >> 5;
  const int rt = xcd * 8 + (idx >> 2), cg = idx & 3;
  const int kq = wid & 3, tg = wid >> 2;
  const char* asrc = ws + OF_OFF + ((size_t)rt * 40 + kq * 10) * 1024 + lane * 16;
  const char* bsrc = ws + WOF_OFF + ((size_t)kq * 10 * 20 + cg * 5 + tg * 3) * 1024 + lane * 16;
  LDSP char* pb = lds + kq * PART_SZ + r * PART_ROW + hh * 16 + tg * 3 * 128;
  if (tg == 0) k3_part<3>(asrc, bsrc, pb); else k3_part<2>(asrc, bsrc, pb);
  __syncthreads();
  for (int it = tid; it < 640; it += 512) {
    const int row = it / 20, c8 = it - row * 20;
    const size_t go = (size_t)(rt * 32 + row) * 640 + cg * 160 + c8 * 8;
    const u32x4 bv = *(const u32x4*)(ws + B_OFF + go * 2);
    const float g = *(const float*)(ws + G_OFF + (size_t)(rt * 32 + row) * 4);
    const LDSP char* p = lds + row * PART_ROW + c8 * 32;
    f32x4 s0 = *(const LDSP f32x4*)(p), s1 = *(const LDSP f32x4*)(p + 16);
#pragma unroll
    for (int w = 1; w < 4; ++w) {
      s0 += *(const LDSP f32x4*)(p + w * PART_SZ);
      s1 += *(const LDSP f32x4*)(p + w * PART_SZ + 16);
    }
    const float h = 1.f - g;
    u32x4 o;
    o[0] = pk2(g * s0[0] + h * bf_lo(bv[0]), g * s0[1] + h * bf_hi(bv[0]));
    o[1] = pk2(g * s0[2] + h * bf_lo(bv[1]), g * s0[3] + h * bf_hi(bv[1]));
    o[2] = pk2(g * s1[0] + h * bf_lo(bv[2]), g * s1[1] + h * bf_hi(bv[2]));
    o[3] = pk2(g * s1[2] + h * bf_lo(bv[3]), g * s1[3] + h * bf_hi(bv[3]));
    *(u32x4*)(out + go) = o;
  }
}

extern "C" void kernel_launch(void* const* d_in, const int* in_sizes, int n_in, void* d_out, int out_size, void* d_ws,
                              size_t ws_size, hipStream_t stream) {
  const u16* x = (const u16*)d_in[0];
  const u16* wqkv = (const u16*)d_in[1];
  const u16* wo = (const u16*)d_in[2];
  const u16* w1 = (const u16*)d_in[4];
  const u16* w2 = (const u16*)d_in[5];
  const u16* gw = (const u16*)d_in[6];
  char* ws = (char*)d_ws;
  k1_kernel<<<256, 512, 0, stream>>>(x, wqkv, w1, w2, wo, gw, ws);
  k2_kernel<<<256, 768, 0, stream>>>(ws);
  k3_kernel<<<256, 512, 0, stream>>>(ws, (u16*)d_out);
}
```

```cpp
#include <hip/hip_runtime.h>
#include <hip/hip_bf16.h>
#include <stdint.h>

typedef __attribute__((ext_vector_type(8))) short bf16x8;
typedef __attribute__((ext_vector_type(4))) short bf16x4;
typedef __attribute__((ext_vector_type(16))) float f32x16;
typedef __attribute__((ext_vector_type(4))) float f32x4;
typedef __attribute__((ext_vector_type(2))) float f32x2;
typedef __attribute__((ext_vector_type(4))) unsigned u32x4;
typedef __attribute__((ext_vector_type(2))) unsigned u32x2;
typedef __attribute__((ext_vector_type(2))) __bf16 bf16x2_t;
typedef unsigned short u16;
#define LDSP __attribute__((address_space(3)))
#define DI __device__ __forceinline__

constexpr int SEQ = 2048, DM = 640, NH = 32, DFF = 1728;
constexpr int KV_STAGE = 21504;
constexpr int KV_VOFF = 10240;
constexpr size_t QF_OFF = 0;
constexpr size_t KV_OFF = QF_OFF + (size_t)32 * 64 * 1280;
constexpr size_t HF_OFF = KV_OFF + (size_t)32 * 8 * KV_STAGE;
constexpr size_t W2F_OFF = HF_OFF + (size_t)64 * 108 * 1024;
constexpr size_t WOF_OFF = W2F_OFF + (size_t)108 * 20 * 1024;
constexpr size_t OF_OFF = WOF_OFF + (size_t)40 * 20 * 1024;
constexpr size_t B_OFF = OF_OFF + (size_t)64 * 40 * 1024;
constexpr size_t G_OFF = B_OFF + (size_t)2048 * 640 * 2;

#define STAMP(kid, slot)
#define DIAGWS
#define MFMA32(a, b, c) __builtin_amdgcn_mfma_f32_32x32x16_bf16((a), (b), (c), 0, 0, 0)
#define MFMA32K8(a, b, c) __builtin_amdgcn_mfma_f32_32x32x8bf16_1k((a), (b), (c), 0, 0, 0)

DI unsigned pk2(float lo, float hi) {
  f32x2 v = {lo, hi};
  bf16x2_t r = __builtin_convertvector(v, bf16x2_t);
  return __builtin_bit_cast(unsigned, r);
}
DI float bf_lo(unsigned u) { return __uint_as_float(u << 16); }
DI float bf_hi(unsigned u) { return __uint_as_float(u & 0xffff0000u); }

DI void glds16(const void* g, unsigned lds_dst) {
  unsigned sv;
  asm volatile("s_mov_b32 %0, m0\n\ts_mov_b32 m0, %2\n\ts_nop 0\n\tglobal_load_lds_dwordx4 %1, off\n\ts_mov_b32 m0, %0"
               : "=&s"(sv) : "v"(g), "s"(lds_dst) : "memory");
}
DI void glds16s(const void* sbase, unsigned voff, unsigned lds_dst) {
  unsigned sv;
  asm volatile("s_mov_b32 %0, m0\n\ts_mov_b32 m0, %3\n\ts_nop 0\n\tglobal_load_lds_dwordx4 %1, %2\n\ts_mov_b32 m0, %0"
               : "=&s"(sv) : "v"(voff), "s"(sbase), "s"(lds_dst) : "memory");
}
typedef __amdgpu_buffer_rsrc_t rsrc_t;
DI rsrc_t mk_rsrc(const void* p) { return __builtin_amdgcn_make_buffer_rsrc((void*)p, 0, 0x7fffffff, 0x00020000); }
DI bf16x8 bload(rsrc_t r, unsigned voff, unsigned soff) {
  return __builtin_bit_cast(bf16x8, __builtin_amdgcn_raw_buffer_load_b128(r, voff, soff, 0));
}
#define STAUX 0
DI void st16(rsrc_t r, unsigned off, u32x4 v) { __builtin_amdgcn_raw_buffer_store_b128(v, r, off, 0, STAUX); }
DI void st8(rsrc_t r, unsigned off, u32x2 v) { __builtin_amdgcn_raw_buffer_store_b64(v, r, off, 0, STAUX); }
DI bf16x4 tr_read(const LDSP char* p) {
  return __builtin_amdgcn_ds_read_tr16_b64_v4i16((LDSP bf16x4*)p);
}
#define WAITV(n) asm volatile("s_waitcnt vmcnt(" #n ")" ::: "memory")
#define BARRIER() asm volatile("s_waitcnt lgkmcnt(0)\n\ts_barrier" ::: "memory")

constexpr int K1_SLOT = 49152;

template <bool TR>
DI void k1_mainloop(f32x16 (&acc)[2][2], LDSP char* lds, const u16* (&asrc)[4], const u16* (&bsrc)[2], int ldw,
                    unsigned lds_a_dst, unsigned lds_b_dst, const int (&aoff)[4], const int (&boff)[2]
                    ) {
  auto issue = [&](int kt, int slot) {
#pragma unroll
    for (int i = 0; i < 4; ++i) glds16(asrc[i] + kt * 64, lds_a_dst + slot * K1_SLOT + i * 1024);
#pragma unroll
    for (int i = 0; i < 2; ++i) glds16(bsrc[i] + (size_t)kt * 64 * ldw, lds_b_dst + slot * K1_SLOT + i * 1024);
  };
  issue(0, 0);
  issue(1, 1);
#pragma unroll
  for (int kt = 0; kt < 10; ++kt) {
    if (kt < 9) WAITV(6); else WAITV(0);
    BARRIER();
    if (kt + 2 < 10) issue(kt + 2, (kt + 2) % 3);
    const LDSP char* sa = lds + (kt % 3) * K1_SLOT;
    const LDSP char* sb = sa + 32768;
#pragma unroll
    for (int ks = 0; ks < 4; ++ks) {
      bf16x8 a[2], b[2];
#pragma unroll
      for (int mt = 0; mt < 2; ++mt) a[mt] = *(const LDSP bf16x8*)(sa + aoff[ks] + mt * 4096);
#pragma unroll
      for (int nt = 0; nt < 2; ++nt) {
        bf16x4 lo = tr_read(sb + boff[nt] + ks * 4096);
        bf16x4 hi = tr_read(sb + boff[nt] + ks * 4096 + 1024);
        b[nt] = __builtin_shufflevector(lo, hi, 0, 1, 2, 3, 4, 5, 6, 7);
      }
#pragma unroll
      for (int mt = 0; mt < 2; ++mt)
#pragma unroll
        for (int nt = 0; nt < 2; ++nt)
          acc[mt][nt] = TR ? MFMA32(b[nt], a[mt], acc[mt][nt]) : MFMA32(a[mt], b[nt], acc[mt][nt]);
    }
  }
}

DI void k1_aux(int aux, const u16* x, const u16* w2, const u16* wo, const u16* gw, char* ws) {
  const int tid = threadIdx.x;
  const rsrc_t rws = mk_rsrc(ws);
  for (int u = aux * 512 + tid; u < 23680; u += 24 * 512) {
    const bool isw2 = u < 17280;
    const int v = isw2 ? u : u - 17280;
    const int co = v % 80, t = v / 80, hh = t & 1, ks = t >> 1;
    const u16* src = isw2 ? w2 : wo;
    u32x4 R[8];
#pragma unroll
    for (int j = 0; j < 8; ++j) {
      int row;
      if (isw2) row = 16 * ks + 8 * (j >> 2) + 4 * hh + (j & 3);
      else if (ks < 32) row = 20 * ks + 8 * (j >> 2) + 4 * hh + (j & 3);
      else row = 20 * (4 * (ks - 32) + 2 * hh + (j >> 2)) + 16 + (j & 3);
      R[j] = *(const u32x4*)(src + (size_t)row * 640 + co * 8);
    }
    const unsigned dst = (unsigned)(isw2 ? W2F_OFF : WOF_OFF) + (ks * 20 + (co >> 2)) * 1024 + hh * 512 + (co & 3) * 128;
#pragma unroll
    for (int c = 0; c < 8; ++c) {
      u32x4 o;
#pragma unroll
      for (int jj = 0; jj < 4; ++jj)
        o[jj] = __builtin_amdgcn_perm(R[2 * jj + 1][c >> 1], R[2 * jj][c >> 1], (c & 1) ? 0x07060302u : 0x05040100u);
      st16(rws, dst + c * 16, o);
    }
  }
  const int wv = aux * 8 + (tid >> 6), lane = tid & 63, sub = lane >> 4, l16 = lane & 15;
  float gwf[5][8];
#pragma unroll
  for (int c = 0; c < 5; ++c) {
    u32x4 g4 = *(const u32x4*)(gw + (c * 16 + l16) * 8);
#pragma unroll
    for (int e = 0; e < 4; ++e) { gwf[c][2 * e] = bf_lo(g4[e]); gwf[c][2 * e + 1] = bf_hi(g4[e]); }
  }
  float* gout = (float*)(ws + G_OFF);
  for (int r0 = wv * 4; r0 < SEQ; r0 += 192 * 4) {
    const int row = r0 + sub;
    float s = 0.f;
#pragma unroll
    for (int c = 0; c < 5; ++c) {
      u32x4 x4 = *(const u32x4*)(x + (size_t)row * 640 + (c * 16 + l16) * 8);
#pragma unroll
      for (int e = 0; e < 4; ++e) { s += bf_lo(x4[e]) * gwf[c][2 * e]; s += bf_hi(x4[e]) * gwf[c][2 * e + 1]; }
    }
    s += __shfl_xor(s, 1); s += __shfl_xor(s, 2); s += __shfl_xor(s, 4); s += __shfl_xor(s, 8);
    if (l16 == 0) gout[row] = 1.f / (1.f + __expf(-s));
  }
}

__global__ __launch_bounds__(512) void k1_kernel(const u16* __restrict__ x, const u16* __restrict__ wqkv,
                                                 const u16* __restrict__ w1, const u16* __restrict__ w2,
                                                 const u16* __restrict__ wo, const u16* __restrict__ gw,
                                                 char* __restrict__ ws) {
  __shared__ __attribute__((aligned(16))) char lds_raw[3 * K1_SLOT];
  const int bid = blockIdx.x, xcd = bid & 7, idx = bid >> 3;
  STAMP(0, 0);
  if (idx >= 29) { k1_aux(xcd * 3 + (idx - 29), x, w2, wo, gw, ws); STAMP(0, 2); return; }
  const int ii = (xcd & 3) * 29 + idx, nti = ii >> 2, mti = (xcd >> 2) * 4 + (ii & 3);
  const int tid = threadIdx.x, lane = tid & 63, wid = __builtin_amdgcn_readfirstlane(tid >> 6);
  const int r = lane & 31, hh = lane >> 5, wm = wid >> 1, wn = wid & 1;
  const int m0 = mti * 256;
  const bool isq = nti < 15;
  const u16* W = isq ? wqkv : w1;
  const int ldw = isq ? 1920 : DFF;
  const int n0 = isq ? nti * 128 : (nti - 15) * 128;
  const bool half_tile = (nti == 28);
  LDSP char* lds = (LDSP char*)lds_raw;
  const unsigned lds0 = (unsigned)(uintptr_t)lds;

  const u16* asrc[4]; const u16* bsrc[2];
#pragma unroll
  for (int i = 0; i < 4; ++i) {
    const int row = 8 * (wid * 4 + i) + (lane >> 3);
    const int c = (lane & 7) ^ ((row >> 1) & 7);
    asrc[i] = x + (size_t)(m0 + row) * 640 + c * 8;
  }
#pragma unroll
  for (int i = 0; i < 2; ++i) {
    const int row = 4 * (wid * 2 + i) + (lane >> 4);
    int c = (lane & 15) ^ ((row & 3) << 2);
    if (half_tile) c &= 7;
    bsrc[i] = W + (size_t)row * ldw + n0 + c * 8;
  }
  int aoff[4], boff[2];
#pragma unroll
  for (int ks = 0; ks < 4; ++ks) aoff[ks] = (wm * 64 + r) * 128 + (((2 * ks + hh) ^ ((r >> 1) & 7)) * 16);
  {
    const int q = (lane & 15) >> 2, p = lane & 3, blk = (lane >> 4) & 1;
#pragma unroll
    for (int nt = 0; nt < 2; ++nt)
      boff[nt] = (8 * hh + q) * 256 + ((((wn * 2 + nt) ^ q) * 4 + 2 * blk + (p >> 1)) * 16) + 8 * (p & 1);
  }
  f32x16 acc[2][2];
#pragma unroll
  for (int mt = 0; mt < 2; ++mt)
#pragma unroll
    for (int nt = 0; nt < 2; ++nt)
#pragma unroll
      for (int e = 0; e < 16; ++e) acc[mt][nt][e] = 0.f;

  const unsigned lds_a_dst = lds0 + wid * 4096, lds_b_dst = lds0 + 32768 + wid * 2048;
  const bool isv = (nti >= 10 && nti < 15);
  const rsrc_t rws = mk_rsrc(ws);
  if (isv) {
    k1_mainloop<false>(acc, lds, asrc, bsrc, ldw, lds_a_dst, lds_b_dst, aoff, boff DIAGWS);
    STAMP(0, 1);
#pragma unroll
    for (int nt = 0; nt < 2; ++nt) {
      const int col = (nti - 10) * 128 + wn * 64 + nt * 32 + r;
      const int head = col / 20, d = col - head * 20;
#pragma unroll
      for (int mt = 0; mt < 2; ++mt) {
        const int rowbase = m0 + wm * 64 + mt * 32;
        const unsigned dst = (unsigned)KV_OFF + (head * 8 + (rowbase >> 8)) * KV_STAGE + KV_VOFF + ((rowbase & 255) >> 5) * 1344 +
                    hh * 336 + d * 16;
#pragma unroll
        for (int s = 0; s < 2; ++s) {
          u32x4 o;
#pragma unroll
          for (int e = 0; e < 4; ++e) o[e] = pk2(acc[mt][nt][8 * s + 2 * e], acc[mt][nt][8 * s + 2 * e + 1]);
          st16(rws, dst + s * 672, o);
          if (d == 19) { u32x4 ones = {0x3f803f80u, 0x3f803f80u, 0x3f803f80u, 0x3f803f80u}; st16(rws, dst + s * 672 + 16, ones); }
        }
      }
    }
  } else {
    k1_mainloop<true>(acc, lds, asrc, bsrc, ldw, lds_a_dst, lds_b_dst, aoff, boff DIAGWS);
    STAMP(0, 1);
    if (nti < 10) {
      const bool q_ = nti < 5;
      const float sc = q_ ? 0.22360679774997896f * 1.4426950408889634f : 1.f;
#pragma unroll
      for (int nt = 0; nt < 2; ++nt) {
        const int c0 = (q_ ? nti : nti - 5) * 128 + wn * 64 + nt * 32;
#pragma unroll
        for (int g = 0; g < 4; ++g) {
          const int G = (c0 >> 2) + 2 * g + hh, head = G / 5, gi = G - head * 5;
          const int inner = gi < 4 ? (gi >> 1) * 512 + r * 16 + (gi & 1) * 8 : 1024 + r * 8;
#pragma unroll
          for (int mt = 0; mt < 2; ++mt) {
            const int rowbase = m0 + wm * 64 + mt * 32;
            const unsigned dst = q_ ? (unsigned)QF_OFF + (head * 64 + (rowbase >> 5)) * 1280 + inner
                                    : (unsigned)KV_OFF + (head * 8 + (rowbase >> 8)) * KV_STAGE + ((rowbase & 255) >> 5) * 1280 + inner;
            u32x2 o;
            o[0] = pk2(acc[mt][nt][4 * g] * sc, acc[mt][nt][4 * g + 1] * sc);
            o[1] = pk2(acc[mt][nt][4 * g + 2] * sc, acc[mt][nt][4 * g + 3] * sc);
            st8(rws, dst, o);
          }
        }
      }
    } else if (!(half_tile && wn == 1)) {
#pragma unroll
      for (int nt = 0; nt < 2; ++nt) {
        const int ks0 = ((nti - 15) * 128 + wn * 64 + nt * 32) >> 4;
#pragma unroll
        for (int mt = 0; mt < 2; ++mt) {
          const int rt = (m0 + wm * 64 + mt * 32) >> 5;
#pragma unroll
          for (int s = 0; s < 2; ++s) {
            u32x4 o;
#pragma unroll
            for (int e = 0; e < 4; ++e)
              o[e] = pk2(fmaxf(acc[mt][nt][8 * s + 2 * e], 0.f), fmaxf(acc[mt][nt][8 * s + 2 * e + 1], 0.f));
            st16(rws, (unsigned)HF_OFF + (rt * 108 + ks0 + s) * 1024 + lane * 16, o);
          }
        }
      }
    }
  }
  STAMP(0, 2);
}

constexpr int K2_RING = 3 * KV_STAGE;
constexpr int PART_ROW = 656;
constexpr int PART_SZ = 32 * PART_ROW;
constexpr int K2_LDS = K2_RING + 4 * PART_SZ;

DI void pack_p(const f32x16& s, bf16x8& p0, bf16x8& p1) {
  u32x4 a, b;
#pragma unroll
  for (int e = 0; e < 4; ++e) { a[e] = pk2(s[2 * e], s[2 * e + 1]); b[e] = pk2(s[8 + 2 * e], s[8 + 2 * e + 1]); }
  p0 = __builtin_bit_cast(bf16x8, a); p1 = __builtin_bit_cast(bf16x8, b);
}
DI float xhalf_max(float m) { return fmaxf(m, __shfl_xor(m, 32)); }

DI void attn_fallback(const char* kvh, bf16x8 q0, bf16x4 q1, int lane, f32x16& o_out) {
  const int r = lane & 31, hh = lane >> 5, rv = r < 20 ? r : 20;
  f32x16 o, zero;
#pragma unroll
  for (int e = 0; e < 16; ++e) { o[e] = 0.f; zero[e] = 0.f; }
  float m = -INFINITY;
  for (int t = 0; t < 64; ++t) {
    const char* st = kvh + (size_t)(t >> 3) * KV_STAGE;
    const int tt = t & 7;
    bf16x8 kf0 = *(const bf16x8*)(st + tt * 1280 + lane * 16);
    bf16x4 kf1 = *(const bf16x4*)(st + tt * 1280 + 1024 + r * 8);
    bf16x8 vf0 = *(const bf16x8*)(st + KV_VOFF + tt * 1344 + hh * 336 + rv * 16);
    bf16x8 vf1 = *(const bf16x8*)(st + KV_VOFF + tt * 1344 + 672 + hh * 336 + rv * 16);
    f32x16 s = MFMA32(kf0, q0, zero);
    s = MFMA32K8(kf1, q1, s);
    float tm = s[0];
#pragma unroll
    for (int e = 1; e < 16; ++e) tm = fmaxf(tm, s[e]);
    tm = xhalf_max(tm);
    const float mn = fmaxf(m, tm);
    const float alpha = __builtin_amdgcn_exp2f(m - mn);
    m = mn;
#pragma unroll
    for (int e = 0; e < 16; ++e) { o[e] *= alpha; s[e] = __builtin_amdgcn_exp2f(s[e] - mn); }
    bf16x8 p0, p1;
    pack_p(s, p0, p1);
    o = MFMA32(vf0, p0, o);
    o = MFMA32(vf1, p1, o);
  }
  o_out = o;
}

__global__ __launch_bounds__(768) void k2_kernel(char* __restrict__ ws) {
  __shared__ __attribute__((aligned(16))) char lds_raw[K2_LDS];
  LDSP char* lds = (LDSP char*)lds_raw;
  const unsigned lds0 = (unsigned)(uintptr_t)lds;
  const int bid = blockIdx.x, xcd = bid & 7, idx = bid >> 3;
  const int tid = threadIdx.x, lane = tid & 63, wid = __builtin_amdgcn_readfirstlane(tid >> 6);
  const int r = lane & 31, hh = lane >> 5;
  STAMP(1, 0);
  if (wid < 8) {
    const int head = xcd * 4 + (idx >> 3), qt = idx & 7;
    const char* kvh = ws + KV_OFF + (size_t)head * 8 * KV_STAGE;
    const int rt = qt * 8 + wid;
    const char* qsrc = ws + QF_OFF + (size_t)(head * 64 + rt) * 1280;
    const bf16x8 q0 = *(const bf16x8*)(qsrc + lane * 16);
    bf16x4 q1 = *(const bf16x4*)(qsrc + 1024 + r * 8);
    if (hh) q1 = bf16x4{0, 0, 0, 0};
    const int rv = r < 20 ? r : 20;
    const int koff0 = lane * 16, koff1 = 1024 + r * 8, voff = KV_VOFF + hh * 336 + rv * 16;
    f32x16 o, negm;
#pragma unroll
    for (int e = 0; e < 16; ++e) { o[e] = 0.f; negm[e] = 0.f; }
    BARRIER();
    STAMP(1, 1);
    bf16x8 kf0 = *(const LDSP bf16x8*)(lds + koff0);
    bf16x4 kf1 = *(const LDSP bf16x4*)(lds + koff1);
    {
      f32x16 s = MFMA32(kf0, q0, negm);
      s = MFMA32K8(kf1, q1, s);
      float tm = s[0];
#pragma unroll
      for (int e = 1; e < 16; ++e) tm = fmaxf(tm, s[e]);
      tm = xhalf_max(tm);
#pragma unroll
      for (int e = 0; e < 16; ++e) negm[e] = -tm;
    }
    f32x16 sA = MFMA32(kf0, q0, negm), sB;
    sA = MFMA32K8(kf1, q1, sA);
    kf0 = *(const LDSP bf16x8*)(lds + 1280 + koff0);
    kf1 = *(const LDSP bf16x4*)(lds + 1280 + koff1);
    u32x4 p0, p1 = {0u, 0u, 0u, 0u};
    bf16x8 vf0, vf1 = {0, 0, 0, 0, 0, 0, 0, 0};
#define SB() __builtin_amdgcn_sched_barrier(0)
#define PIN(x) asm volatile("" : "+v"(x))
#define EX(S, i) S[i] = __builtin_amdgcn_exp2f(S[i])
#define TILE(SC, SN, VP, KP)                                                                   \
  do {                                                                                         \
    EX(SC, 0); EX(SC, 1); SB();                                                                \
    o = MFMA32(vf1, __builtin_bit_cast(bf16x8, p1), o); SB();                                  \
    vf0 = *(const LDSP bf16x8*)((VP)); vf1 = *(const LDSP bf16x8*)((VP) + 672);                \
    EX(SC, 2); EX(SC, 3); p0[0] = pk2(SC[0], SC[1]); p0[1] = pk2(SC[2], SC[3]);                \
    EX(SC, 4); EX(SC, 5); SB();                                                                \
    SN = MFMA32(kf0, q0, negm); SB();                                                          \
    EX(SC, 6); EX(SC, 7); p0[2] = pk2(SC[4], SC[5]); p0[3] = pk2(SC[6], SC[7]);                \
    EX(SC, 8); EX(SC, 9); SB();                                                                \
    SN = MFMA32K8(kf1, q1, SN); SB();                                                          \
    kf0 = *(const LDSP bf16x8*)((KP) + koff0); kf1 = *(const LDSP bf16x4*)((KP) + koff1);      \
    EX(SC, 10); EX(SC, 11); p1[0] = pk2(SC[8], SC[9]); p1[1] = pk2(SC[10], SC[11]);            \
    EX(SC, 12); EX(SC, 13); SB();                                                              \
    o = MFMA32(vf0, __builtin_bit_cast(bf16x8, p0), o); SB();                                  \
    EX(SC, 14); EX(SC, 15); p1[2] = pk2(SC[12], SC[13]); p1[3] = pk2(SC[14], SC[15]); SB();    \
  } while (0)
    int slot = 0;
    for (int stage = 0; stage < 8; ++stage) {
      const int nslot = slot == 2 ? 0 : slot + 1;
      const LDSP char* cb = lds + slot * KV_STAGE;
      const LDSP char* nb = lds + nslot * KV_STAGE;
      const LDSP char* vb = cb + voff;
      TILE(sA, sB, vb + 0 * 1344, cb + 2 * 1280);
      TILE(sB, sA, vb + 1 * 1344, cb + 3 * 1280);
      TILE(sA, sB, vb + 2 * 1344, cb + 4 * 1280);
      TILE(sB, sA, vb + 3 * 1344, cb + 5 * 1280);
      TILE(sA, sB, vb + 4 * 1344, cb + 6 * 1280);
      TILE(sB, sA, vb + 5 * 1344, cb + 7 * 1280);
      STAMP(1, 2 + stage);
      if (stage < 7) asm volatile("s_barrier" ::: "memory");
      TILE(sA, sB, vb + 6 * 1344, nb + 0 * 1280);
      TILE(sB, sA, vb + 7 * 1344, nb + 1 * 1280);
      slot = nslot;
    }
    o = MFMA32(vf1, __builtin_bit_cast(bf16x8, p1), o);
    {
      float l = __shfl(o[8], 32 + r);
      float amax = 0.f;
#pragma unroll
      for (int e = 0; e < 12; ++e) amax = fmaxf(amax, fabsf(o[e]));
      bool bad = !(l <= 1.8446744e19f) || !(amax <= 3.0e38f);
#pragma unroll
      for (int e = 0; e < 12; ++e) bad = bad || (o[e] != o[e]);
      if (__any(bad)) attn_fallback(kvh, q0, q1, lane, o);
    }
    const float l = __shfl(o[8], 32 + r);
    const float inv = 1.f / l;
    const rsrc_t rws = mk_rsrc(ws);
    const unsigned odst = (unsigned)OF_OFF + rt * 40 * 1024;
    u32x4 om;
#pragma unroll
    for (int e = 0; e < 4; ++e) om[e] = pk2(o[2 * e] * inv, o[2 * e + 1] * inv);
    st16(rws, odst + head * 1024 + lane * 16, om);
    if (hh == 0) {
      u32x2 orr;
      orr[0] = pk2(o[8] * inv, o[9] * inv);
      orr[1] = pk2(o[10] * inv, o[11] * inv);
      st8(rws, odst + (32 + (head >> 2)) * 1024 + (((head >> 1) & 1) * 32 + r) * 16 + (head & 1) * 8, orr);
    }
    STAMP(1, 10);
  } else {
#define GPRIO 0
#define GSLEEP 0
    __builtin_amdgcn_s_setprio(GPRIO);
    const int kq = wid - 8;
    const int rt = xcd * 8 + (idx >> 2), cg = idx & 3;
    const rsrc_t ra = mk_rsrc(ws + HF_OFF + ((size_t)rt * 108 + kq * 27) * 1024);
    const rsrc_t rb = mk_rsrc(ws + W2F_OFF + ((size_t)kq * 27 * 20 + cg * 5) * 1024);
    const unsigned voff = lane * 16;
    const int head = xcd * 4 + (idx >> 3);
    const char* dsrc = ws + KV_OFF + (size_t)head * 8 * KV_STAGE + kq * 1024;
    auto dma = [&](int stage) {
      const char* s = dsrc + (size_t)stage * KV_STAGE;
      const unsigned d = lds0 + (stage % 3) * KV_STAGE + kq * 1024;
#pragma unroll
      for (int i = 0; i < 5; ++i) glds16s(s + i * 4096, voff, d + i * 4096);
      glds16s(s + 20480 - kq * 1024, voff, d + 20480 - kq * 1024);
    };
    dma(0);
    dma(1);
    f32x16 acc[5];
#pragma unroll
    for (int c = 0; c < 5; ++c)
#pragma unroll
      for (int e = 0; e < 16; ++e) acc[c][e] = 0.f;
    constexpr int PD = 3;
    bf16x8 af[PD], bfr[PD][5];
#pragma unroll
    for (int j = 0; j < PD; ++j) {
      af[j] = bload(ra, voff, j * 1024);
#pragma unroll
      for (int c = 0; c < 5; ++c) bfr[j][c] = bload(rb, voff, j * 20480 + c * 1024);
    }
    __builtin_amdgcn_sched_barrier(0);
    WAITV(24);
    BARRIER();
    __builtin_amdgcn_sched_barrier(0);
#pragma unroll
    for (int stage = 0; stage < 7; ++stage) {
#pragma unroll
      for (int i = 0; i < 4; ++i) {
        const int k = stage * 4 + i;
        if (k < 27) {
          const int j = k % PD;
#pragma unroll
          for (int c = 0; c < 5; ++c) {
            acc[c] = MFMA32(bfr[j][c], af[j], acc[c]);
            if (GSLEEP) { __builtin_amdgcn_sched_barrier(0); __builtin_amdgcn_s_sleep(GSLEEP); __builtin_amdgcn_sched_barrier(0); }
          }
          __builtin_amdgcn_sched_barrier(0);
          if (k + PD < 27) {
            af[j] = bload(ra, voff, (k + PD) * 1024);
#pragma unroll
            for (int c = 0; c < 5; ++c) bfr[j][c] = bload(rb, voff, (k + PD) * 20480 + c * 1024);
          }
          __builtin_amdgcn_sched_barrier(0);
        }
      }
      if (stage == 6) {
        unsigned vz = voff;
        asm volatile("" : "+v"(vz));
        const int l2 = vz >> 4;
        LDSP char* pb = lds + K2_RING + kq * PART_SZ + (l2 & 31) * PART_ROW + (l2 >> 5) * 16;
#pragma unroll
        for (int c = 0; c < 5; ++c)
#pragma unroll
          for (int g = 0; g < 4; ++g) {
            f32x4 v = {acc[c][4 * g], acc[c][4 * g + 1], acc[c][4 * g + 2], acc[c][4 * g + 3]};
            *(LDSP f32x4*)(pb + c * 128 + g * 32) = v;
          }
      }
      STAMP(1, 2 + stage);
      if (stage < 6) WAITV(18); else WAITV(0);
      BARRIER();
      if (stage + 2 < 8) dma(stage + 2);
      __builtin_amdgcn_sched_barrier(0);
    }
    unsigned vy = voff;
    asm volatile("" : "+v"(vy));
    for (int it = kq * 64 + (vy >> 4); it < 640; it += 256) {
      const int row = it / 20, c8 = it - row * 20;
      const LDSP char* pb = lds + K2_RING + row * PART_ROW + c8 * 32;
      f32x4 s0 = *(const LDSP f32x4*)(pb), s1 = *(const LDSP f32x4*)(pb + 16);
#pragma unroll
      for (int w = 1; w < 4; ++w) {
        s0 += *(const LDSP f32x4*)(pb + w * PART_SZ);
        s1 += *(const LDSP f32x4*)(pb + w * PART_SZ + 16);
      }
      u32x4 o = {pk2(s0[0], s0[1]), pk2(s0[2], s0[3]), pk2(s1[0], s1[1]), pk2(s1[2], s1[3])};
      st16(mk_rsrc(ws), (unsigned)B_OFF + ((rt * 32 + row) * 640 + cg * 160 + c8 * 8) * 2, o);
    }
    STAMP(1, 10);
  }
}

template <int NT>
DI void k3_part(const char* asrc, const char* bsrc, LDSP char* pb) {
  bf16x8 af[10], bfr[10][NT];
#pragma unroll
  for (int k = 0; k < 10; ++k) {
    af[k] = *(const bf16x8*)(asrc + k * 1024);
#pragma unroll
    for (int c = 0; c < NT; ++c) bfr[k][c] = *(const bf16x8*)(bsrc + (size_t)k * 20480 + c * 1024);
  }
  __builtin_amdgcn_sched_barrier(0);
  f32x16 acc[NT];
#pragma unroll
  for (int c = 0; c < NT; ++c)
#pragma unroll
    for (int e = 0; e < 16; ++e) acc[c][e] = 0.f;
#pragma unroll
  for (int k = 0; k < 10; ++k)
#pragma unroll
    for (int c = 0; c < NT; ++c) acc[c] = MFMA32(bfr[k][c], af[k], acc[c]);
#pragma unroll
  for (int c = 0; c < NT; ++c)
#pragma unroll
    for (int g = 0; g < 4; ++g) {
      f32x4 v = {acc[c][4 * g], acc[c][4 * g + 1], acc[c][4 * g + 2], acc[c][4 * g + 3]};
      *(LDSP f32x4*)(pb + c * 128 + g * 32) = v;
    }
}

__global__ __launch_bounds__(512) void k3_kernel(const char* __restrict__ ws, u16* __restrict__ out) {
  __shared__ __attribute__((aligned(16))) char lds_raw[4 * PART_SZ];
  LDSP char* lds = (LDSP char*)lds_raw;
  const int bid = blockIdx.x, xcd = bid & 7, idx = bid >> 3;
  const int tid = threadIdx.x, lane = tid & 63, wid = __builtin_amdgcn_readfirstlane(tid >> 6);
  const int r = lane & 31, hh = lane >> 5;
  const int rt = xcd * 8 + (idx >> 2), cg = idx & 3;
  const int kq = wid & 3, tg = wid >> 2;
  STAMP(2, 0);
  const char* asrc = ws + OF_OFF + ((size_t)rt * 40 + kq * 10) * 1024 + lane * 16;
  const char* bsrc = ws + WOF_OFF + ((size_t)kq * 10 * 20 + cg * 5 + tg * 3) * 1024 + lane * 16;
  LDSP char* pb = lds + kq * PART_SZ + r * PART_ROW + hh * 16 + tg * 3 * 128;
  if (tg == 0) k3_part<3>(asrc, bsrc, pb); else k3_part<2>(asrc, bsrc, pb);
  STAMP(2, 1);
  __syncthreads();
  for (int it = tid; it < 640; it += 512) {
    const int row = it / 20, c8 = it - row * 20;
    const size_t go = (size_t)(rt * 32 + row) * 640 + cg * 160 + c8 * 8;
    const u32x4 bv = *(const u32x4*)(ws + B_OFF + go * 2);
    const float g = *(const float*)(ws + G_OFF + (size_t)(rt * 32 + row) * 4);
    const LDSP char* p = lds + row * PART_ROW + c8 * 32;
    f32x4 s0 = *(const LDSP f32x4*)(p), s1 = *(const LDSP f32x4*)(p + 16);
#pragma unroll
    for (int w = 1; w < 4; ++w) {
      s0 += *(const LDSP f32x4*)(p + w * PART_SZ);
      s1 += *(const LDSP f32x4*)(p + w * PART_SZ + 16);
    }
    const float h = 1.f - g;
    u32x4 o;
    o[0] = pk2(g * s0[0] + h * bf_lo(bv[0]), g * s0[1] + h * bf_hi(bv[0]));
    o[1] = pk2(g * s0[2] + h * bf_lo(bv[1]), g * s0[3] + h * bf_hi(bv[1]));
    o[2] = pk2(g * s1[0] + h * bf_lo(bv[2]), g * s1[1] + h * bf_hi(bv[2]));
    o[3] = pk2(g * s1[2] + h * bf_lo(bv[3]), g * s1[3] + h * bf_hi(bv[3]));
    *(u32x4*)(out + go) = o;
  }
  STAMP(2, 2);
}

extern "C" void kernel_launch(void* const* d_in, const int* in_sizes, int n_in, void* d_out, int out_size, void* d_ws,
                              size_t ws_size, hipStream_t stream) {
  const u16* x = (const u16*)d_in[0];
  const u16* wqkv = (const u16*)d_in[1];
  const u16* wo = (const u16*)d_in[2];
  const u16* w1 = (const u16*)d_in[4];
  const u16* w2 = (const u16*)d_in[5];
  const u16* gw = (const u16*)d_in[6];
  char* ws = (char*)d_ws;
  k1_kernel<<<256, 512, 0, stream>>>(x, wqkv, w1, w2, wo, gw, ws);
  k2_kernel<<<256, 768, 0, stream>>>(ws);
  k3_kernel<<<256, 512, 0, stream>>>(ws, (u16*)d_out);
}
```

```cpp
#include <hip/hip_runtime.h>
#include <hip/hip_bf16.h>
#include <stdint.h>

typedef __attribute__((ext_vector_type(8))) short bf16x8;
typedef __attribute__((ext_vector_type(4))) short bf16x4;
typedef __attribute__((ext_vector_type(16))) float f32x16;
typedef __attribute__((ext_vector_type(4))) float f32x4;
typedef __attribute__((ext_vector_type(2))) float f32x2;
typedef __attribute__((ext_vector_type(4))) unsigned u32x4;
typedef __attribute__((ext_vector_type(2))) unsigned u32x2;
typedef __attribute__((ext_vector_type(2))) __bf16 bf16x2_t;
typedef unsigned short u16;
#define LDSP __attribute__((address_space(3)))
#define DI __device__ __forceinline__

constexpr int SEQ = 2048, DM = 640, NH = 32, DFF = 1728;
constexpr int KV_STAGE = 21504;
constexpr int KV_VOFF = 10240;
constexpr size_t QF_OFF = 0;
constexpr size_t KV_OFF = QF_OFF + (size_t)32 * 64 * 1280;
constexpr size_t HF_OFF = KV_OFF + (size_t)32 * 8 * KV_STAGE;
constexpr size_t W2F_OFF = HF_OFF + (size_t)64 * 108 * 1024;
constexpr size_t WOF_OFF = W2F_OFF + (size_t)108 * 20 * 1024;
constexpr size_t OF_OFF = WOF_OFF + (size_t)40 * 20 * 1024;
constexpr size_t G_OFF = OF_OFF + (size_t)64 * 40 * 1024;
constexpr size_t CNT_OFF = G_OFF + 2048 * 4;

#define TK(i)
#define TKARG
#define TKPASS
#define STAMP(kid, slot)
#define DIAGWS
#define MFMA32(a, b, c) __builtin_amdgcn_mfma_f32_32x32x16_bf16((a), (b), (c), 0, 0, 0)
#define MFMA32K8(a, b, c) __builtin_amdgcn_mfma_f32_32x32x8bf16_1k((a), (b), (c), 0, 0, 0)

DI unsigned pk2(float lo, float hi) {
  f32x2 v = {lo, hi};
  bf16x2_t r = __builtin_convertvector(v, bf16x2_t);
  return __builtin_bit_cast(unsigned, r);
}
DI float bf_lo(unsigned u) { return __uint_as_float(u << 16); }
DI float bf_hi(unsigned u) { return __uint_as_float(u & 0xffff0000u); }

DI void glds16(const void* g, unsigned lds_dst) {
  unsigned sv;
  asm volatile("s_mov_b32 %0, m0\n\ts_mov_b32 m0, %2\n\ts_nop 0\n\tglobal_load_lds_dwordx4 %1, off\n\ts_mov_b32 m0, %0"
               : "=&s"(sv) : "v"(g), "s"(lds_dst) : "memory");
}
DI void glds16s(const void* sbase, unsigned voff, unsigned lds_dst) {
  unsigned sv;
  asm volatile("s_mov_b32 %0, m0\n\ts_mov_b32 m0, %3\n\ts_nop 0\n\tglobal_load_lds_dwordx4 %1, %2\n\ts_mov_b32 m0, %0"
               : "=&s"(sv) : "v"(voff), "s"(sbase), "s"(lds_dst) : "memory");
}
typedef __amdgpu_buffer_rsrc_t rsrc_t;
DI rsrc_t mk_rsrc(const void* p) { return __builtin_amdgcn_make_buffer_rsrc((void*)p, 0, 0x7fffffff, 0x00020000); }
DI bf16x8 bload(rsrc_t r, unsigned voff, unsigned soff) {
  return __builtin_bit_cast(bf16x8, __builtin_amdgcn_raw_buffer_load_b128(r, voff, soff, 0));
}
#define STAUX 0
DI void st16(rsrc_t r, unsigned off, u32x4 v) { __builtin_amdgcn_raw_buffer_store_b128(v, r, off, 0, STAUX); }
#define STAUXH 0
DI void st16h(rsrc_t r, unsigned off, u32x4 v) { __builtin_amdgcn_raw_buffer_store_b128(v, r, off, 0, STAUXH); }
DI void st8(rsrc_t r, unsigned off, u32x2 v) { __builtin_amdgcn_raw_buffer_store_b64(v, r, off, 0, STAUX); }
DI bf16x4 tr_read(const LDSP char* p) {
  return __builtin_amdgcn_ds_read_tr16_b64_v4i16((LDSP bf16x4*)p);
}
#define WAITV(n) asm volatile("s_waitcnt vmcnt(" #n ")" ::: "memory")
#define BARRIER() asm volatile("s_waitcnt lgkmcnt(0)\n\ts_barrier" ::: "memory")

constexpr int K1_SLOT = 49152;

template <bool TR>
DI void k1_mainloop(f32x16 (&acc)[2][2], LDSP char* lds, const int (&aoff)[4], const int (&boff)[2]
                    ) {
  bf16x8 fa[2][2], fb[2][2];
  auto rd = [&](int buf, int kt, int ks) {
    const LDSP char* sa = lds + (kt % 3) * K1_SLOT;
    const LDSP char* sb = sa + 32768;
#pragma unroll
    for (int nt = 0; nt < 2; ++nt) {
      bf16x4 lo = tr_read(sb + boff[nt] + ks * 4096);
      bf16x4 hi = tr_read(sb + boff[nt] + ks * 4096 + 1024);
      fb[buf][nt] = __builtin_shufflevector(lo, hi, 0, 1, 2, 3, 4, 5, 6, 7);
    }
#pragma unroll
    for (int mt = 0; mt < 2; ++mt) fa[buf][mt] = *(const LDSP bf16x8*)(sa + aoff[ks] + mt * 4096);
  };
  BARRIER();
  rd(0, 0, 0);
  __builtin_amdgcn_sched_barrier(0);
#pragma unroll
  for (int kt = 0; kt < 10; ++kt) {
#pragma unroll
    for (int ks = 0; ks < 4; ++ks) {
      const int cb = ks & 1;
      if (ks < 3) rd(cb ^ 1, kt, ks + 1);
      else if (kt < 9) {
        BARRIER();
        rd(cb ^ 1, kt + 1, 0);
      }
      __builtin_amdgcn_sched_barrier(0);
#pragma unroll
      for (int mt = 0; mt < 2; ++mt)
#pragma unroll
        for (int nt = 0; nt < 2; ++nt)
          acc[mt][nt] = TR ? MFMA32(fb[cb][nt], fa[cb][mt], acc[mt][nt]) : MFMA32(fa[cb][mt], fb[cb][nt], acc[mt][nt]);
      __builtin_amdgcn_sched_barrier(0);
    }
  }
}

DI void k1_loader(int lw, int lane, unsigned lds0, const u16* xa, const u16* wb, int ldw, bool half_tile) {
  unsigned aoffs[2], boffs;
#pragma unroll
  for (int par = 0; par < 2; ++par) aoffs[par] = (lane >> 3) * 1280 + (((lane & 7) ^ ((4 * par + (lane >> 4)) & 7)) * 16);
  {
    int c = (lane & 15) ^ (((lane >> 4) & 3) << 2);
    if (half_tile) c &= 7;
    boffs = (lane >> 4) * ldw * 2 + c * 16;
  }
  auto issue = [&](int kt) {
    const unsigned sl = lds0 + (kt % 3) * K1_SLOT;
#pragma unroll
    for (int i = 0; i < 8; ++i) {
      const int pc = lw * 8 + i;
      glds16s(xa + (size_t)pc * 8 * 640 + kt * 64, aoffs[i & 1], sl + pc * 1024);
    }
#pragma unroll
    for (int i = 0; i < 4; ++i) {
      const int pc = lw * 4 + i;
      glds16s(wb + (size_t)(kt * 64 + pc * 4) * ldw, boffs, sl + 32768 + pc * 1024);
    }
  };
  issue(0);
  issue(1);
  WAITV(12);
  BARRIER();
  issue(2);
#pragma unroll
  for (int kt = 0; kt < 9; ++kt) {
    if (kt < 8) WAITV(12); else WAITV(0);
    BARRIER();
    if (kt + 3 < 10) issue(kt + 3);
  }
}

DI void k1_aux(int aux, LDSP char* lds, const u16* x, const u16* w2, const u16* wo, const u16* gw, char* ws) {
  const int tid = threadIdx.x, lane = tid & 63, wid = __builtin_amdgcn_readfirstlane(tid >> 6);
  const unsigned lds0 = (unsigned)(uintptr_t)lds;
  const rsrc_t rws = mk_rsrc(ws);
  if (aux == 0 && tid < 8) *(unsigned*)(ws + CNT_OFF + tid * 256) = 0u;
  constexpr int SLAB = 16 * 1344;
  for (int pc = wid; pc < 147; pc += 12) {
    const int sl = pc / 21, pp = pc - sl * 21;
    int ksg = aux + sl * 24;
    if (ksg >= 148) ksg = 147;
    const int i = pp * 64 + lane, rho = i / 84;
    int c = i - rho * 84;
    if (c > 79) c = 79;
    const bool isw2 = ksg < 108;
    const int ks = isw2 ? ksg : ksg - 108;
    int row;
    if (isw2) row = 16 * ks + rho;
    else if (ks < 32) row = 20 * ks + rho;
    else row = 20 * (4 * (ks - 32) + 2 * ((rho >> 2) & 1) + (rho >> 3)) + 16 + (rho & 3);
    glds16((isw2 ? w2 : wo) + (size_t)row * 640 + c * 8, lds0 + pc * 1024);
  }
  const int wv = aux * 12 + wid, sub = lane >> 4, l16 = lane & 15;
  u32x4 g4[5], x4[2][5];
#pragma unroll
  for (int c = 0; c < 5; ++c) g4[c] = *(const u32x4*)(gw + (c * 16 + l16) * 8);
#pragma unroll
  for (int i = 0; i < 2; ++i) {
    int row = wv * 4 + i * 1152 + sub;
    if (row >= SEQ) row = SEQ - 1;
#pragma unroll
    for (int c = 0; c < 5; ++c) x4[i][c] = *(const u32x4*)(x + (size_t)row * 640 + (c * 16 + l16) * 8);
  }
  float* gout = (float*)(ws + G_OFF);
#pragma unroll
  for (int i = 0; i < 2; ++i) {
    const int row = wv * 4 + i * 1152 + sub;
    float s = 0.f;
#pragma unroll
    for (int c = 0; c < 5; ++c)
#pragma unroll
      for (int e = 0; e < 4; ++e) { s += bf_lo(x4[i][c][e]) * bf_lo(g4[c][e]); s += bf_hi(x4[i][c][e]) * bf_hi(g4[c][e]); }
    s += __shfl_xor(s, 1); s += __shfl_xor(s, 2); s += __shfl_xor(s, 4); s += __shfl_xor(s, 8);
    if (l16 == 0 && row < SEQ) gout[row] = 1.f / (1.f + __expf(-s));
  }
  WAITV(0);
  BARRIER();
  const int toff = (4 * (lane >> 5) + ((lane & 15) >> 2)) * 1344 + ((lane >> 4) & 1) * 32 + (lane & 3) * 8;
  for (int f = wid; f < 140; f += 12) {
    const int sl = f / 20, ct = f - sl * 20;
    const int ksg = aux + sl * 24;
    if (ksg >= 148) break;
    const LDSP char* pb = lds + sl * SLAB + toff + ct * 64;
    bf16x4 lo = tr_read(pb), hi = tr_read(pb + 8 * 1344);
    bf16x8 v = __builtin_shufflevector(lo, hi, 0, 1, 2, 3, 4, 5, 6, 7);
    st16(rws, (unsigned)W2F_OFF + (ksg * 20 + ct) * 1024 + lane * 16, __builtin_bit_cast(u32x4, v));
  }
}

__global__ __launch_bounds__(768) void k1_kernel(const u16* __restrict__ x, const u16* __restrict__ wqkv,
                                                 const u16* __restrict__ w1, const u16* __restrict__ w2,
                                                 const u16* __restrict__ wo, const u16* __restrict__ gw,
                                                 char* __restrict__ ws) {
  __shared__ __attribute__((aligned(16))) char lds_raw[147 * 1024];
  const int bid = blockIdx.x, xcd = bid & 7, idx = bid >> 3;
  STAMP(0, 0);
  if (idx >= 29) { k1_aux(xcd * 3 + (idx - 29), (LDSP char*)lds_raw, x, w2, wo, gw, ws); STAMP(0, 2); return; }
  const int ii = (xcd & 3) * 29 + idx, nti = ii >> 2, mti = (xcd >> 2) * 4 + (ii & 3);
  const int tid = threadIdx.x, lane = tid & 63, wid = __builtin_amdgcn_readfirstlane(tid >> 6);
  const int r = lane & 31, hh = lane >> 5, wm = wid >> 1, wn = wid & 1;
  const int m0 = mti * 256;
  const bool isq = nti < 15;
  const u16* W = isq ? wqkv : w1;
  const int ldw = isq ? 1920 : DFF;
  const int n0 = isq ? nti * 128 : (nti - 15) * 128;
  const bool half_tile = (nti == 28);
  LDSP char* lds = (LDSP char*)lds_raw;
  const unsigned lds0 = (unsigned)(uintptr_t)lds;

  if (wid >= 8) { k1_loader(wid - 8, lane, lds0, x + (size_t)m0 * 640, W + n0, ldw, half_tile); return; }
  int aoff[4], boff[2];
#pragma unroll
  for (int ks = 0; ks < 4; ++ks) aoff[ks] = (wm * 64 + r) * 128 + (((2 * ks + hh) ^ ((r >> 1) & 7)) * 16);
  {
    const int q = (lane & 15) >> 2, p = lane & 3, blk = (lane >> 4) & 1;
#pragma unroll
    for (int nt = 0; nt < 2; ++nt)
      boff[nt] = (8 * hh + q) * 256 + ((((wn * 2 + nt) ^ q) * 4 + 2 * blk + (p >> 1)) * 16) + 8 * (p & 1);
  }
  f32x16 acc[2][2];
#pragma unroll
  for (int mt = 0; mt < 2; ++mt)
#pragma unroll
    for (int nt = 0; nt < 2; ++nt)
#pragma unroll
      for (int e = 0; e < 16; ++e) acc[mt][nt][e] = 0.f;

  const bool isv = (nti >= 10 && nti < 15);
  const rsrc_t rws = mk_rsrc(ws);
  if (isv) {
    k1_mainloop<false>(acc, lds, aoff, boff DIAGWS);
    STAMP(0, 1);
#pragma unroll
    for (int nt = 0; nt < 2; ++nt) {
      const int col = (nti - 10) * 128 + wn * 64 + nt * 32 + r;
      const int head = col / 20, d = col - head * 20;
#pragma unroll
      for (int mt = 0; mt < 2; ++mt) {
        const int rowbase = m0 + wm * 64 + mt * 32;
        const unsigned dst = (unsigned)KV_OFF + (head * 8 + (rowbase >> 8)) * KV_STAGE + KV_VOFF + ((rowbase & 255) >> 5) * 1344 +
                    hh * 336 + d * 16;
#pragma unroll
        for (int s = 0; s < 2; ++s) {
          u32x4 o;
#pragma unroll
          for (int e = 0; e < 4; ++e) o[e] = pk2(acc[mt][nt][8 * s + 2 * e], acc[mt][nt][8 * s + 2 * e + 1]);
          st16(rws, dst + s * 672, o);
          if (d == 19) { u32x4 ones = {0x3f803f80u, 0x3f803f80u, 0x3f803f80u, 0x3f803f80u}; st16(rws, dst + s * 672 + 16, ones); }
        }
      }
    }
  } else {
    k1_mainloop<true>(acc, lds, aoff, boff DIAGWS);
    STAMP(0, 1);
    if (nti < 10) {
      const bool q_ = nti < 5;
      const float sc = q_ ? 0.22360679774997896f * 1.4426950408889634f : 1.f;
#pragma unroll
      for (int nt = 0; nt < 2; ++nt) {
        const int c0 = (q_ ? nti : nti - 5) * 128 + wn * 64 + nt * 32;
#pragma unroll
        for (int g = 0; g < 4; ++g) {
          const int G = (c0 >> 2) + 2 * g + hh, head = G / 5, gi = G - head * 5;
          const int inner = gi < 4 ? (gi >> 1) * 512 + r * 16 + (gi & 1) * 8 : 1024 + r * 8;
#pragma unroll
          for (int mt = 0; mt < 2; ++mt) {
            const int rowbase = m0 + wm * 64 + mt * 32;
            const unsigned dst = q_ ? (unsigned)QF_OFF + (head * 64 + (rowbase >> 5)) * 1280 + inner
                                    : (unsigned)KV_OFF + (head * 8 + (rowbase >> 8)) * KV_STAGE + ((rowbase & 255) >> 5) * 1280 + inner;
            u32x2 o;
            o[0] = pk2(acc[mt][nt][4 * g] * sc, acc[mt][nt][4 * g + 1] * sc);
            o[1] = pk2(acc[mt][nt][4 * g + 2] * sc, acc[mt][nt][4 * g + 3] * sc);
            st8(rws, dst, o);
          }
        }
      }
    } else if (!(half_tile && wn == 1)) {
#pragma unroll
      for (int nt = 0; nt < 2; ++nt) {
        const int ks0 = ((nti - 15) * 128 + wn * 64 + nt * 32) >> 4;
#pragma unroll
        for (int mt = 0; mt < 2; ++mt) {
          const int rt = (m0 + wm * 64 + mt * 32) >> 5;
#pragma unroll
          for (int s = 0; s < 2; ++s) {
            u32x4 o;
#pragma unroll
            for (int e = 0; e < 4; ++e)
              o[e] = pk2(fmaxf(acc[mt][nt][8 * s + 2 * e], 0.f), fmaxf(acc[mt][nt][8 * s + 2 * e + 1], 0.f));
            st16h(rws, (unsigned)HF_OFF + (rt * 108 + ks0 + s) * 1024 + lane * 16, o);
          }
        }
      }
    }
  }
  STAMP(0, 2);
}

constexpr int K2_RING = 3 * KV_STAGE;
constexpr int PART_ROW = 656;
constexpr int PART_SZ = 32 * PART_ROW;
constexpr int K2_LDS = K2_RING + 4 * PART_SZ;

DI void pack_p(const f32x16& s, bf16x8& p0, bf16x8& p1) {
  u32x4 a, b;
#pragma unroll
  for (int e = 0; e < 4; ++e) { a[e] = pk2(s[2 * e], s[2 * e + 1]); b[e] = pk2(s[8 + 2 * e], s[8 + 2 * e + 1]); }
  p0 = __builtin_bit_cast(bf16x8, a); p1 = __builtin_bit_cast(bf16x8, b);
}
DI float xhalf_max(float m) { return fmaxf(m, __shfl_xor(m, 32)); }

DI void attn_fallback(const char* kvh, bf16x8 q0, bf16x4 q1, int lane, f32x16& o_out) {
  const int r = lane & 31, hh = lane >> 5, rv = r < 20 ? r : 20;
  f32x16 o, zero;
#pragma unroll
  for (int e = 0; e < 16; ++e) { o[e] = 0.f; zero[e] = 0.f; }
  float m = -INFINITY;
  for (int t = 0; t < 64; ++t) {
    const char* st = kvh + (size_t)(t >> 3) * KV_STAGE;
    const int tt = t & 7;
    bf16x8 kf0 = *(const bf16x8*)(st + tt * 1280 + lane * 16);
    bf16x4 kf1 = *(const bf16x4*)(st + tt * 1280 + 1024 + r * 8);
    bf16x8 vf0 = *(const bf16x8*)(st + KV_VOFF + tt * 1344 + hh * 336 + rv * 16);
    bf16x8 vf1 = *(const bf16x8*)(st + KV_VOFF + tt * 1344 + 672 + hh * 336 + rv * 16);
    f32x16 s = MFMA32(kf0, q0, zero);
    s = MFMA32K8(kf1, q1, s);
    float tm = s[0];
#pragma unroll
    for (int e = 1; e < 16; ++e) tm = fmaxf(tm, s[e]);
    tm = xhalf_max(tm);
    const float mn = fmaxf(m, tm);
    const float alpha = __builtin_amdgcn_exp2f(m - mn);
    m = mn;
#pragma unroll
    for (int e = 0; e < 16; ++e) { o[e] *= alpha; s[e] = __builtin_amdgcn_exp2f(s[e] - mn); }
    bf16x8 p0, p1;
    pack_p(s, p0, p1);
    o = MFMA32(vf0, p0, o);
    o = MFMA32(vf1, p1, o);
  }
  o_out = o;
}

template <int NT, bool SIGNAL>
DI void oproj(rsrc_t ro, rsrc_t rw, unsigned voff, LDSP char* pb, unsigned* sig, const unsigned* cnt TKARG) {
  bf16x8 bfr[10][NT], af[10];
  asm volatile("" ::: "memory");
  __builtin_amdgcn_sched_barrier(0);
#pragma unroll
  for (int k = 0; k < 10; ++k)
#pragma unroll
    for (int c = 0; c < NT; ++c) bfr[k][c] = bload(rw, voff, k * 20480 + c * 1024);
  __builtin_amdgcn_sched_barrier(0);
  if (!SIGNAL) WAITV(20);
  TK(11);
  BARRIER();
  TK(12);
  if (SIGNAL && sig != nullptr) {
    __hip_atomic_fetch_add(sig, 1u, __ATOMIC_RELAXED, __HIP_MEMORY_SCOPE_AGENT);
    for (int spin = 0; spin < (1 << 22); ++spin) {
      if (__hip_atomic_load(cnt, __ATOMIC_RELAXED, __HIP_MEMORY_SCOPE_AGENT) >= 32u) break;
      __builtin_amdgcn_s_sleep(1);
    }
  }
  BARRIER();
  TK(13);
#pragma unroll
  for (int k = 0; k < 10; ++k)
    af[k] = __builtin_bit_cast(bf16x8, __builtin_amdgcn_raw_buffer_load_b128(ro, voff, k * 1024, 16));
  __builtin_amdgcn_sched_barrier(0);
  f32x16 acc[NT];
#pragma unroll
  for (int c = 0; c < NT; ++c)
#pragma unroll
    for (int e = 0; e < 16; ++e) acc[c][e] = 0.f;
#pragma unroll
  for (int k = 0; k < 10; ++k)
#pragma unroll
    for (int c = 0; c < NT; ++c) acc[c] = MFMA32(bfr[k][c], af[k], acc[c]);
#pragma unroll
  for (int c = 0; c < NT; ++c)
#pragma unroll
    for (int g = 0; g < 4; ++g) {
      f32x4 v = {acc[c][4 * g], acc[c][4 * g + 1], acc[c][4 * g + 2], acc[c][4 * g + 3]};
      *(LDSP f32x4*)(pb + c * 128 + g * 32) = v;
    }
}

__global__ __launch_bounds__(768) void k2_kernel(char* __restrict__ ws, u16* __restrict__ out) {
  __shared__ __attribute__((aligned(16))) char lds_raw[K2_LDS];
  LDSP char* lds = (LDSP char*)lds_raw;
  const unsigned lds0 = (unsigned)(uintptr_t)lds;
  const int bid = blockIdx.x, xcd = bid & 7, idx = bid >> 3;
  const int tid = threadIdx.x, lane = tid & 63, wid = __builtin_amdgcn_readfirstlane(tid >> 6);
  const int r = lane & 31, hh = lane >> 5;
  STAMP(1, 0);
  if (wid < 8) {
    const int head = xcd * 4 + (idx >> 3), qt = idx & 7;
    const char* kvh = ws + KV_OFF + (size_t)head * 8 * KV_STAGE;
    const int rt = qt * 8 + wid;
    const char* qsrc = ws + QF_OFF + (size_t)(head * 64 + rt) * 1280;
    const bf16x8 q0 = *(const bf16x8*)(qsrc + lane * 16);
    bf16x4 q1 = *(const bf16x4*)(qsrc + 1024 + r * 8);
    if (hh) q1 = bf16x4{0, 0, 0, 0};
    const int rv = r < 20 ? r : 20;
    const int koff0 = lane * 16, koff1 = 1024 + r * 8, voff = KV_VOFF + hh * 336 + rv * 16;
    f32x16 o, negm;
#pragma unroll
    for (int e = 0; e < 16; ++e) { o[e] = 0.f; negm[e] = 0.f; }
    BARRIER();
    TK(1);
    bf16x8 kf0 = *(const LDSP bf16x8*)(lds + koff0);
    bf16x4 kf1 = *(const LDSP bf16x4*)(lds + koff1);
    {
      f32x16 s = MFMA32(kf0, q0, negm);
      s = MFMA32K8(kf1, q1, s);
      float tm = s[0];
#pragma unroll
      for (int e = 1; e < 16; ++e) tm = fmaxf(tm, s[e]);
      tm = xhalf_max(tm);
#pragma unroll
      for (int e = 0; e < 16; ++e) negm[e] = -tm;
    }
    f32x16 sA = MFMA32(kf0, q0, negm), sB;
    sA = MFMA32K8(kf1, q1, sA);
    kf0 = *(const LDSP bf16x8*)(lds + 1280 + koff0);
    kf1 = *(const LDSP bf16x4*)(lds + 1280 + koff1);
    u32x4 p0, p1 = {0u, 0u, 0u, 0u};
    bf16x8 vf0, vf1 = {0, 0, 0, 0, 0, 0, 0, 0};
#define SB() __builtin_amdgcn_sched_barrier(0)
#define PIN(x) asm volatile("" : "+v"(x))
#define EX(S, i) S[i] = __builtin_amdgcn_exp2f(S[i])
#define TILE(SC, SN, VP, KP)                                                                   \
  do {                                                                                         \
    EX(SC, 0); EX(SC, 1); SB();                                                                \
    o = MFMA32(vf1, __builtin_bit_cast(bf16x8, p1), o); SB();                                  \
    vf0 = *(const LDSP bf16x8*)((VP)); vf1 = *(const LDSP bf16x8*)((VP) + 672);                \
    EX(SC, 2); EX(SC, 3); p0[0] = pk2(SC[0], SC[1]); p0[1] = pk2(SC[2], SC[3]);                \
    EX(SC, 4); EX(SC, 5); SB();                                                                \
    SN = MFMA32(kf0, q0, negm); SB();                                                          \
    EX(SC, 6); EX(SC, 7); p0[2] = pk2(SC[4], SC[5]); p0[3] = pk2(SC[6], SC[7]);                \
    EX(SC, 8); EX(SC, 9); SB();                                                                \
    SN = MFMA32K8(kf1, q1, SN); SB();                                                          \
    kf0 = *(const LDSP bf16x8*)((KP) + koff0); kf1 = *(const LDSP bf16x4*)((KP) + koff1);      \
    EX(SC, 10); EX(SC, 11); p1[0] = pk2(SC[8], SC[9]); p1[1] = pk2(SC[10], SC[11]);            \
    EX(SC, 12); EX(SC, 13); SB();                                                              \
    o = MFMA32(vf0, __builtin_bit_cast(bf16x8, p0), o); SB();                                  \
    EX(SC, 14); EX(SC, 15); p1[2] = pk2(SC[12], SC[13]); p1[3] = pk2(SC[14], SC[15]); SB();    \
  } while (0)
    int slot = 0;
    for (int stage = 0; stage < 8; ++stage) {
      const int nslot = slot == 2 ? 0 : slot + 1;
      const LDSP char* cb = lds + slot * KV_STAGE;
      const LDSP char* nb = lds + nslot * KV_STAGE;
      const LDSP char* vb = cb + voff;
      TILE(sA, sB, vb + 0 * 1344, cb + 2 * 1280);
      TILE(sB, sA, vb + 1 * 1344, cb + 3 * 1280);
      TILE(sA, sB, vb + 2 * 1344, cb + 4 * 1280);
      TILE(sB, sA, vb + 3 * 1344, cb + 5 * 1280);
      TILE(sA, sB, vb + 4 * 1344, cb + 6 * 1280);
      TILE(sB, sA, vb + 5 * 1344, cb + 7 * 1280);
      if (stage == 0) TK(2); if (stage == 3) TK(3); if (stage == 6) TK(4); if (stage == 7) TK(5);
      if (stage < 7) asm volatile("s_barrier" ::: "memory");
      TILE(sA, sB, vb + 6 * 1344, nb + 0 * 1280);
      TILE(sB, sA, vb + 7 * 1344, nb + 1 * 1280);
      slot = nslot;
    }
    o = MFMA32(vf1, __builtin_bit_cast(bf16x8, p1), o);
    {
      float l = __shfl(o[8], 32 + r);
      float amax = 0.f;
#pragma unroll
      for (int e = 0; e < 12; ++e) amax = fmaxf(amax, fabsf(o[e]));
      bool bad = !(l <= 1.8446744e19f) || !(amax <= 3.0e38f);
#pragma unroll
      for (int e = 0; e < 12; ++e) bad = bad || (o[e] != o[e]);
      if (__any(bad)) attn_fallback(kvh, q0, q1, lane, o);
    }
    const float l = __shfl(o[8], 32 + r);
    const float inv = 1.f / l;
    const rsrc_t rws = mk_rsrc(ws);
    const unsigned odst = (unsigned)OF_OFF + rt * 40 * 1024;
    u32x4 om;
#pragma unroll
    for (int e = 0; e < 4; ++e) om[e] = pk2(o[2 * e] * inv, o[2 * e + 1] * inv);
    __builtin_amdgcn_raw_buffer_store_b128(om, rws, odst + head * 1024 + lane * 16, 0, 16);
    if (hh == 0) {
      u32x2 orr;
      orr[0] = pk2(o[8] * inv, o[9] * inv);
      orr[1] = pk2(o[10] * inv, o[11] * inv);
      __builtin_amdgcn_raw_buffer_store_b64(orr, rws, odst + (32 + (head >> 2)) * 1024 + (((head >> 1) & 1) * 32 + r) * 16 + (head & 1) * 8, 0, 16);
    }
    TK(10);
    const int jrt = xcd * 8 + (idx >> 2), cg = idx & 3, kq = wid & 3, tg = wid >> 2;
    const rsrc_t ro = mk_rsrc(ws + OF_OFF + ((size_t)jrt * 40 + kq * 10) * 1024);
    const rsrc_t rw = mk_rsrc(ws + WOF_OFF + ((size_t)kq * 10 * 20 + cg * 5 + tg * 2) * 1024);
    LDSP char* pb = lds + K2_RING + kq * PART_SZ + r * PART_ROW + hh * 16 + tg * 2 * 128;
    oproj<2, false>(ro, rw, lane * 16, pb, nullptr, nullptr TKPASS);
  } else {
#define GPRIO 0
#define GSLEEP 0
    __builtin_amdgcn_s_setprio(GPRIO);
    const int kq = wid - 8;
    const int rt = xcd * 8 + (idx >> 2), cg = idx & 3;
    const rsrc_t ra = mk_rsrc(ws + HF_OFF + ((size_t)rt * 108 + kq * 27) * 1024);
    const rsrc_t rb = mk_rsrc(ws + W2F_OFF + ((size_t)kq * 27 * 20 + cg * 5) * 1024);
    const unsigned voff = lane * 16;
    const int head = xcd * 4 + (idx >> 3);
    const char* dsrc = ws + KV_OFF + (size_t)head * 8 * KV_STAGE + kq * 1024;
    auto dma = [&](int stage) {
      const char* s = dsrc + (size_t)stage * KV_STAGE;
      const unsigned d = lds0 + (stage % 3) * KV_STAGE + kq * 1024;
#pragma unroll
      for (int i = 0; i < 5; ++i) glds16s(s + i * 4096, voff, d + i * 4096);
      glds16s(s + 20480 - kq * 1024, voff, d + 20480 - kq * 1024);
    };
    dma(0);
    dma(1);
    f32x16 acc[5];
#pragma unroll
    for (int c = 0; c < 5; ++c)
#pragma unroll
      for (int e = 0; e < 16; ++e) acc[c][e] = 0.f;
    constexpr int PD = 3;
    bf16x8 af[PD], bfr[PD][5];
#pragma unroll
    for (int j = 0; j < PD; ++j) {
      af[j] = bload(ra, voff, j * 1024);
#pragma unroll
      for (int c = 0; c < 5; ++c) bfr[j][c] = bload(rb, voff, j * 20480 + c * 1024);
    }
    __builtin_amdgcn_sched_barrier(0);
    WAITV(24);
    BARRIER();
    __builtin_amdgcn_sched_barrier(0);
#pragma unroll
    for (int stage = 0; stage < 7; ++stage) {
#pragma unroll
      for (int i = 0; i < 4; ++i) {
        const int k = stage * 4 + i;
        if (k < 27) {
          const int j = k % PD;
#pragma unroll
          for (int c = 0; c < 5; ++c) {
            acc[c] = MFMA32(bfr[j][c], af[j], acc[c]);
            if (GSLEEP) { __builtin_amdgcn_sched_barrier(0); __builtin_amdgcn_s_sleep(GSLEEP); __builtin_amdgcn_sched_barrier(0); }
          }
          __builtin_amdgcn_sched_barrier(0);
          if (k + PD < 27) {
            af[j] = bload(ra, voff, (k + PD) * 1024);
#pragma unroll
            for (int c = 0; c < 5; ++c) bfr[j][c] = bload(rb, voff, (k + PD) * 20480 + c * 1024);
          }
          __builtin_amdgcn_sched_barrier(0);
        }
      }
      if (stage == 6) {
        unsigned vz = voff;
        asm volatile("" : "+v"(vz));
        const int l2 = vz >> 4;
        LDSP char* pb = lds + K2_RING + kq * PART_SZ + (l2 & 31) * PART_ROW + (l2 >> 5) * 16;
#pragma unroll
        for (int c = 0; c < 5; ++c)
#pragma unroll
          for (int g = 0; g < 4; ++g) {
            f32x4 v = {acc[c][4 * g], acc[c][4 * g + 1], acc[c][4 * g + 2], acc[c][4 * g + 3]};
            *(LDSP f32x4*)(pb + c * 128 + g * 32) = v;
          }
      }
      TK(2 + stage);
      if (stage < 6) WAITV(18); else WAITV(0);
      BARRIER();
      if (stage + 2 < 8) dma(stage + 2);
      __builtin_amdgcn_sched_barrier(0);
    }
    __builtin_amdgcn_s_setprio(3);
    unsigned vy = voff;
    asm volatile("" : "+v"(vy));
    for (int it = kq * 64 + (vy >> 4); it < 640; it += 256) {
      const int row = it / 20, c8 = it - row * 20;
      const LDSP char* pb = lds + K2_RING + row * PART_ROW + c8 * 32;
      f32x4 s0 = *(const LDSP f32x4*)(pb), s1 = *(const LDSP f32x4*)(pb + 16);
#pragma unroll
      for (int w = 1; w < 4; ++w) {
        s0 += *(const LDSP f32x4*)(pb + w * PART_SZ);
        s1 += *(const LDSP f32x4*)(pb + w * PART_SZ + 16);
      }
      LDSP char* qb = lds + 2 * KV_STAGE + row * PART_ROW + c8 * 32;
      *(LDSP f32x4*)(qb) = s0;
      *(LDSP f32x4*)(qb + 16) = s1;
    }
    TK(10);
    {
      const rsrc_t ro = mk_rsrc(ws + OF_OFF + ((size_t)rt * 40 + kq * 10) * 1024);
      const rsrc_t rw = mk_rsrc(ws + WOF_OFF + ((size_t)kq * 10 * 20 + cg * 5 + 4) * 1024);
      const int l2 = vy >> 4;
      LDSP char* pb = lds + K2_RING + kq * PART_SZ + (l2 & 31) * PART_ROW + (l2 >> 5) * 16 + 4 * 128;
      unsigned* sig = (kq == 0 && vy == 0) ? (unsigned*)(ws + CNT_OFF + (idx & 7) * 256) : nullptr;
      oproj<1, true>(ro, rw, vy, pb, sig, (const unsigned*)(ws + CNT_OFF + xcd * 256) TKPASS);
    }
  }
  BARRIER();
  TK(14);
  {
    const int jrt = xcd * 8 + (idx >> 2), cg = idx & 3;
    if (tid < 640) {
      const int row = tid / 20, c8 = tid - row * 20;
      const float g = *(const float*)(ws + G_OFF + (size_t)(jrt * 32 + row) * 4);
      const LDSP char* p = lds + K2_RING + row * PART_ROW + c8 * 32;
      f32x4 s0 = *(const LDSP f32x4*)(p), s1 = *(const LDSP f32x4*)(p + 16);
#pragma unroll
      for (int w = 1; w < 4; ++w) {
        s0 += *(const LDSP f32x4*)(p + w * PART_SZ);
        s1 += *(const LDSP f32x4*)(p + w * PART_SZ + 16);
      }
      const LDSP char* qb = lds + 2 * KV_STAGE + row * PART_ROW + c8 * 32;
      const f32x4 b0 = *(const LDSP f32x4*)(qb), b1 = *(const LDSP f32x4*)(qb + 16);
      const float h = 1.f - g;
      u32x4 o;
      o[0] = pk2(g * s0[0] + h * b0[0], g * s0[1] + h * b0[1]);
      o[1] = pk2(g * s0[2] + h * b0[2], g * s0[3] + h * b0[3]);
      o[2] = pk2(g * s1[0] + h * b1[0], g * s1[1] + h * b1[1]);
      o[3] = pk2(g * s1[2] + h * b1[2], g * s1[3] + h * b1[3]);
      *(u32x4*)(out + (size_t)(jrt * 32 + row) * 640 + cg * 160 + c8 * 8) = o;
    }
  }
  STAMP(1, 15);
}

extern "C" void kernel_launch(void* const* d_in, const int* in_sizes, int n_in, void* d_out, int out_size, void* d_ws,
                              size_t ws_size, hipStream_t stream) {
  const u16* x = (const u16*)d_in[0];
  const u16* wqkv = (const u16*)d_in[1];
  const u16* wo = (const u16*)d_in[2];
  const u16* w1 = (const u16*)d_in[4];
  const u16* w2 = (const u16*)d_in[5];
  const u16* gw = (const u16*)d_in[6];
  char* ws = (char*)d_ws;
  k1_kernel<<<256, 768, 0, stream>>>(x, wqkv, w1, w2, wo, gw, ws);
  k2_kernel<<<256, 768, 0, stream>>>(ws, (u16*)d_out);
}
```

```cpp
#include <hip/hip_runtime.h>
#include <hip/hip_bf16.h>
#include <stdint.h>

typedef __attribute__((ext_vector_type(8))) short bf16x8;
typedef __attribute__((ext_vector_type(4))) short bf16x4;
typedef __attribute__((ext_vector_type(16))) float f32x16;
typedef __attribute__((ext_vector_type(4))) float f32x4;
typedef __attribute__((ext_vector_type(2))) float f32x2;
typedef __attribute__((ext_vector_type(4))) unsigned u32x4;
typedef __attribute__((ext_vector_type(2))) unsigned u32x2;
typedef __attribute__((ext_vector_type(2))) __bf16 bf16x2_t;
typedef unsigned short u16;
#define LDSP __attribute__((address_space(3)))
#define DI __device__ __forceinline__

constexpr int SEQ = 2048, DM = 640, NH = 32, DFF = 1728;
constexpr int KT = 1296, VT = 1408, VS = 704, VH = 352;
constexpr int KV_VOFF = 8 * KT;
constexpr int KV_STAGE = 22528;
constexpr size_t QF_OFF = 0;
constexpr size_t KV_OFF = QF_OFF + (size_t)32 * 64 * 1280;
constexpr size_t HF_OFF = KV_OFF + (size_t)32 * 8 * KV_STAGE;
constexpr size_t W2F_OFF = HF_OFF + (size_t)64 * 108 * 1024;
constexpr size_t WOF_OFF = W2F_OFF + (size_t)108 * 20 * 1024;
constexpr size_t OF_OFF = WOF_OFF + (size_t)40 * 20 * 1024;
constexpr size_t G_OFF = OF_OFF + (size_t)64 * 40 * 1024;

#define TK(i)
#define TKARG
#define TKPASS
#define STAMP(kid, slot)
#define DIAGWS
#define MFMA32(a, b, c) __builtin_amdgcn_mfma_f32_32x32x16_bf16((a), (b), (c), 0, 0, 0)
#define MFMA32K8(a, b, c) __builtin_amdgcn_mfma_f32_32x32x8bf16_1k((a), (b), (c), 0, 0, 0)

DI unsigned pk2(float lo, float hi) {
  f32x2 v = {lo, hi};
  bf16x2_t r = __builtin_convertvector(v, bf16x2_t);
  return __builtin_bit_cast(unsigned, r);
}
DI float bf_lo(unsigned u) { return __uint_as_float(u << 16); }
DI float bf_hi(unsigned u) { return __uint_as_float(u & 0xffff0000u); }

DI void glds16(const void* g, unsigned lds_dst) {
  unsigned sv;
  asm volatile("s_mov_b32 %0, m0\n\ts_mov_b32 m0, %2\n\ts_nop 0\n\tglobal_load_lds_dwordx4 %1, off\n\ts_mov_b32 m0, %0"
               : "=&s"(sv) : "v"(g), "s"(lds_dst) : "memory");
}
DI void glds16s(const void* sbase, unsigned voff, unsigned lds_dst) {
  unsigned sv;
  asm volatile("s_mov_b32 %0, m0\n\ts_mov_b32 m0, %3\n\ts_nop 0\n\tglobal_load_lds_dwordx4 %1, %2\n\ts_mov_b32 m0, %0"
               : "=&s"(sv) : "v"(voff), "s"(sbase), "s"(lds_dst) : "memory");
}
typedef __amdgpu_buffer_rsrc_t rsrc_t;
DI rsrc_t mk_rsrc(const void* p) { return __builtin_amdgcn_make_buffer_rsrc((void*)p, 0, 0x7fffffff, 0x00020000); }
DI bf16x8 bload(rsrc_t r, unsigned voff, unsigned soff) {
  return __builtin_bit_cast(bf16x8, __builtin_amdgcn_raw_buffer_load_b128(r, voff, soff, 0));
}
#define STAUX 0
DI void st16(rsrc_t r, unsigned off, u32x4 v) { __builtin_amdgcn_raw_buffer_store_b128(v, r, off, 0, STAUX); }
#define STAUXH 0
DI void st16h(rsrc_t r, unsigned off, u32x4 v) { __builtin_amdgcn_raw_buffer_store_b128(v, r, off, 0, STAUXH); }
DI void st8(rsrc_t r, unsigned off, u32x2 v) { __builtin_amdgcn_raw_buffer_store_b64(v, r, off, 0, STAUX); }
DI bf16x4 tr_read(const LDSP char* p) {
  return __builtin_amdgcn_ds_read_tr16_b64_v4i16((LDSP bf16x4*)p);
}
#define WAITV(n) asm volatile("s_waitcnt vmcnt(" #n ")" ::: "memory")
#define BARRIER() asm volatile("s_waitcnt lgkmcnt(0)\n\ts_barrier" ::: "memory")

constexpr int K1_SLOT = 49152;

template <bool TR>
DI void k1_mainloop(f32x16 (&acc)[2][2], LDSP char* lds, const int (&aoff)[4], const int (&boff)[2]
                    ) {
  bf16x8 fa[2][2], fb[2][2];
  auto rd = [&](int buf, int kt, int ks) {
    const LDSP char* sa = lds + (kt % 3) * K1_SLOT;
    const LDSP char* sb = sa + 32768;
#pragma unroll
    for (int nt = 0; nt < 2; ++nt) {
      bf16x4 lo = tr_read(sb + boff[nt] + ks * 4096);
      bf16x4 hi = tr_read(sb + boff[nt] + ks * 4096 + 1024);
      fb[buf][nt] = __builtin_shufflevector(lo, hi, 0, 1, 2, 3, 4, 5, 6, 7);
    }
#pragma unroll
    for (int mt = 0; mt < 2; ++mt) fa[buf][mt] = *(const LDSP bf16x8*)(sa + aoff[ks] + mt * 4096);
  };
  BARRIER();
  rd(0, 0, 0);
  __builtin_amdgcn_sched_barrier(0);
#pragma unroll
  for (int kt = 0; kt < 10; ++kt) {
#pragma unroll
    for (int ks = 0; ks < 4; ++ks) {
      const int cb = ks & 1;
      if (ks < 3) rd(cb ^ 1, kt, ks + 1);
      else if (kt < 9) {
        BARRIER();
        rd(cb ^ 1, kt + 1, 0);
      }
      __builtin_amdgcn_sched_barrier(0);
#pragma unroll
      for (int mt = 0; mt < 2; ++mt)
#pragma unroll
        for (int nt = 0; nt < 2; ++nt)
          acc[mt][nt] = TR ? MFMA32(fb[cb][nt], fa[cb][mt], acc[mt][nt]) : MFMA32(fa[cb][mt], fb[cb][nt], acc[mt][nt]);
      __builtin_amdgcn_sched_barrier(0);
    }
  }
}

DI void k1_loader(int lw, int lane, unsigned lds0, const u16* xa, const u16* wb, int ldw, bool half_tile) {
  unsigned aoffs[2], boffs;
#pragma unroll
  for (int par = 0; par < 2; ++par) aoffs[par] = (lane >> 3) * 1280 + (((lane & 7) ^ ((4 * par + (lane >> 4)) & 7)) * 16);
  {
    int c = (lane & 15) ^ (((lane >> 4) & 3) << 2);
    if (half_tile) c &= 7;
    boffs = (lane >> 4) * ldw * 2 + c * 16;
  }
  auto issue = [&](int kt) {
    const unsigned sl = lds0 + (kt % 3) * K1_SLOT;
#pragma unroll
    for (int i = 0; i < 8; ++i) {
      const int pc = lw * 8 + i;
      glds16s(xa + (size_t)pc * 8 * 640 + kt * 64, aoffs[i & 1], sl + pc * 1024);
    }
#pragma unroll
    for (int i = 0; i < 4; ++i) {
      const int pc = lw * 4 + i;
      glds16s(wb + (size_t)(kt * 64 + pc * 4) * ldw, boffs, sl + 32768 + pc * 1024);
    }
  };
  issue(0);
  issue(1);
  WAITV(12);
  BARRIER();
  issue(2);
#pragma unroll
  for (int kt = 0; kt < 9; ++kt) {
    if (kt < 8) WAITV(12); else WAITV(0);
    BARRIER();
    if (kt + 3 < 10) issue(kt + 3);
  }
}

DI void k1_aux(int aux, LDSP char* lds, const u16* x, const u16* w2, const u16* wo, const u16* gw, char* ws) {
  const int tid = threadIdx.x, lane = tid & 63, wid = __builtin_amdgcn_readfirstlane(tid >> 6);
  const unsigned lds0 = (unsigned)(uintptr_t)lds;
  const rsrc_t rws = mk_rsrc(ws);
  {
    const u32x4 ff = {0xffffffffu, 0xffffffffu, 0xffffffffu, 0xffffffffu};
    for (int i = aux * 768 + tid; i < 64 * 40 * 64; i += 24 * 768) st16(rws, (unsigned)OF_OFF + i * 16, ff);
  }
  constexpr int SLAB = 16 * 1344;
  for (int pc = wid; pc < 147; pc += 12) {
    const int sl = pc / 21, pp = pc - sl * 21;
    int ksg = aux + sl * 24;
    if (ksg >= 148) ksg = 147;
    const int i = pp * 64 + lane, rho = i / 84;
    int c = i - rho * 84;
    if (c > 79) c = 79;
    const bool isw2 = ksg < 108;
    const int ks = isw2 ? ksg : ksg - 108;
    int row;
    if (isw2) row = 16 * ks + rho;
    else if (ks < 32) row = 20 * ks + rho;
    else row = 20 * (4 * (ks - 32) + 2 * ((rho >> 2) & 1) + (rho >> 3)) + 16 + (rho & 3);
    glds16((isw2 ? w2 : wo) + (size_t)row * 640 + c * 8, lds0 + pc * 1024);
  }
  const int wv = aux * 12 + wid, sub = lane >> 4, l16 = lane & 15;
  u32x4 g4[5], x4[2][5];
#pragma unroll
  for (int c = 0; c < 5; ++c) g4[c] = *(const u32x4*)(gw + (c * 16 + l16) * 8);
#pragma unroll
  for (int i = 0; i < 2; ++i) {
    int row = wv * 4 + i * 1152 + sub;
    if (row >= SEQ) row = SEQ - 1;
#pragma unroll
    for (int c = 0; c < 5; ++c) x4[i][c] = *(const u32x4*)(x + (size_t)row * 640 + (c * 16 + l16) * 8);
  }
  float* gout = (float*)(ws + G_OFF);
#pragma unroll
  for (int i = 0; i < 2; ++i) {
    const int row = wv * 4 + i * 1152 + sub;
    float s = 0.f;
#pragma unroll
    for (int c = 0; c < 5; ++c)
#pragma unroll
      for (int e = 0; e < 4; ++e) { s += bf_lo(x4[i][c][e]) * bf_lo(g4[c][e]); s += bf_hi(x4[i][c][e]) * bf_hi(g4[c][e]); }
    s += __shfl_xor(s, 1); s += __shfl_xor(s, 2); s += __shfl_xor(s, 4); s += __shfl_xor(s, 8);
    if (l16 == 0 && row < SEQ) gout[row] = 1.f / (1.f + __expf(-s));
  }
  WAITV(0);
  BARRIER();
  const int toff = (4 * (lane >> 5) + ((lane & 15) >> 2)) * 1344 + ((lane >> 4) & 1) * 32 + (lane & 3) * 8;
  for (int f = wid; f < 140; f += 12) {
    const int sl = f / 20, ct = f - sl * 20;
    const int ksg = aux + sl * 24;
    if (ksg >= 148) break;
    const LDSP char* pb = lds + sl * SLAB + toff + ct * 64;
    bf16x4 lo = tr_read(pb), hi = tr_read(pb + 8 * 1344);
    bf16x8 v = __builtin_shufflevector(lo, hi, 0, 1, 2, 3, 4, 5, 6, 7);
    st16(rws, (unsigned)W2F_OFF + (ksg * 20 + ct) * 1024 + lane * 16, __builtin_bit_cast(u32x4, v));
  }
}

__global__ __launch_bounds__(768) void k1_kernel(const u16* __restrict__ x, const u16* __restrict__ wqkv,
                                                 const u16* __restrict__ w1, const u16* __restrict__ w2,
                                                 const u16* __restrict__ wo, const u16* __restrict__ gw,
                                                 char* __restrict__ ws) {
  __shared__ __attribute__((aligned(16))) char lds_raw[147 * 1024];
  const int bid = blockIdx.x, xcd = bid & 7, idx = bid >> 3;
  STAMP(0, 0);
  if (idx >= 29) { k1_aux(xcd * 3 + (idx - 29), (LDSP char*)lds_raw, x, w2, wo, gw, ws); STAMP(0, 2); return; }
  const int ii = (xcd & 3) * 29 + idx, nti = ii >> 2, mti = (xcd >> 2) * 4 + (ii & 3);
  const int tid = threadIdx.x, lane = tid & 63, wid = __builtin_amdgcn_readfirstlane(tid >> 6);
  const int r = lane & 31, hh = lane >> 5, wm = wid >> 1, wn = wid & 1;
  const int m0 = mti * 256;
  const bool isq = nti < 15;
  const u16* W = isq ? wqkv : w1;
  const int ldw = isq ? 1920 : DFF;
  const int n0 = isq ? nti * 128 : (nti - 15) * 128;
  const bool half_tile = (nti == 28);
  LDSP char* lds = (LDSP char*)lds_raw;
  const unsigned lds0 = (unsigned)(uintptr_t)lds;

  if (wid >= 8) { k1_loader(wid - 8, lane, lds0, x + (size_t)m0 * 640, W + n0, ldw, half_tile); return; }
  int aoff[4], boff[2];
#pragma unroll
  for (int ks = 0; ks < 4; ++ks) aoff[ks] = (wm * 64 + r) * 128 + (((2 * ks + hh) ^ ((r >> 1) & 7)) * 16);
  {
    const int q = (lane & 15) >> 2, p = lane & 3, blk = (lane >> 4) & 1;
#pragma unroll
    for (int nt = 0; nt < 2; ++nt)
      boff[nt] = (8 * hh + q) * 256 + ((((wn * 2 + nt) ^ q) * 4 + 2 * blk + (p >> 1)) * 16) + 8 * (p & 1);
  }
  f32x16 acc[2][2];
#pragma unroll
  for (int mt = 0; mt < 2; ++mt)
#pragma unroll
    for (int nt = 0; nt < 2; ++nt)
#pragma unroll
      for (int e = 0; e < 16; ++e) acc[mt][nt][e] = 0.f;

  const bool isv = (nti >= 10 && nti < 15);
  const rsrc_t rws = mk_rsrc(ws);
  if (isv) {
    k1_mainloop<false>(acc, lds, aoff, boff DIAGWS);
    STAMP(0, 1);
#pragma unroll
    for (int nt = 0; nt < 2; ++nt) {
      const int col = (nti - 10) * 128 + wn * 64 + nt * 32 + r;
      const int head = col / 20, d = col - head * 20;
#pragma unroll
      for (int mt = 0; mt < 2; ++mt) {
        const int rowbase = m0 + wm * 64 + mt * 32;
        const unsigned dst = (unsigned)KV_OFF + (head * 8 + (rowbase >> 8)) * KV_STAGE + KV_VOFF + ((rowbase & 255) >> 5) * VT +
                    hh * VH + d * 16;
#pragma unroll
        for (int s = 0; s < 2; ++s) {
          u32x4 o;
#pragma unroll
          for (int e = 0; e < 4; ++e) o[e] = pk2(acc[mt][nt][8 * s + 2 * e], acc[mt][nt][8 * s + 2 * e + 1]);
          st16(rws, dst + s * VS, o);
          if (d == 19) {
            const u32x4 ones = {0x3f803f80u, 0x3f803f80u, 0x3f803f80u, 0x3f803f80u}, zeros = {0u, 0u, 0u, 0u};
            st16(rws, dst + s * VS + 16, ones);
            st16(rws, dst + s * VS + 32, zeros);
          }
        }
      }
    }
  } else {
    k1_mainloop<true>(acc, lds, aoff, boff DIAGWS);
    STAMP(0, 1);
    if (nti < 10) {
      const bool q_ = nti < 5;
      const float sc = q_ ? 0.22360679774997896f * 1.4426950408889634f : 1.f;
#pragma unroll
      for (int nt = 0; nt < 2; ++nt) {
        const int c0 = (q_ ? nti : nti - 5) * 128 + wn * 64 + nt * 32;
#pragma unroll
        for (int g = 0; g < 4; ++g) {
          const int G = (c0 >> 2) + 2 * g + hh, head = G / 5, gi = G - head * 5;
          const int inner = gi < 4 ? (gi >> 1) * 512 + r * 16 + (gi & 1) * 8 : 1024 + r * 8;
#pragma unroll
          for (int mt = 0; mt < 2; ++mt) {
            const int rowbase = m0 + wm * 64 + mt * 32;
            const unsigned dst = q_ ? (unsigned)QF_OFF + (head * 64 + (rowbase >> 5)) * 1280 + inner
                                    : (unsigned)KV_OFF + (head * 8 + (rowbase >> 8)) * KV_STAGE + ((rowbase & 255) >> 5) * KT + inner;
            u32x2 o;
            o[0] = pk2(acc[mt][nt][4 * g] * sc, acc[mt][nt][4 * g + 1] * sc);
            o[1] = pk2(acc[mt][nt][4 * g + 2] * sc, acc[mt][nt][4 * g + 3] * sc);
            st8(rws, dst, o);
            if (!q_ && gi == 4 && r == 31) st8(rws, dst + 8, u32x2{0x00003f80u, 0u});
          }
        }
      }
    } else if (!(half_tile && wn == 1)) {
#pragma unroll
      for (int nt = 0; nt < 2; ++nt) {
        const int ks0 = ((nti - 15) * 128 + wn * 64 + nt * 32) >> 4;
#pragma unroll
        for (int mt = 0; mt < 2; ++mt) {
          const int rt = (m0 + wm * 64 + mt * 32) >> 5;
#pragma unroll
          for (int s = 0; s < 2; ++s) {
            u32x4 o;
#pragma unroll
            for (int e = 0; e < 4; ++e)
              o[e] = pk2(fmaxf(acc[mt][nt][8 * s + 2 * e], 0.f), fmaxf(acc[mt][nt][8 * s + 2 * e + 1], 0.f));
            st16h(rws, (unsigned)HF_OFF + (rt * 108 + ks0 + s) * 1024 + lane * 16, o);
          }
        }
      }
    }
  }
  STAMP(0, 2);
}

constexpr int K2_RING = 3 * KV_STAGE;
constexpr int PART_ROW = 656;
constexpr int PART_SZ = 32 * PART_ROW;
constexpr int K2_FLAGS = K2_RING + 4 * PART_SZ;
constexpr int K2_LDS = K2_FLAGS + 128;

DI void pack_p(const f32x16& s, bf16x8& p0, bf16x8& p1) {
  u32x4 a, b;
#pragma unroll
  for (int e = 0; e < 4; ++e) { a[e] = pk2(s[2 * e], s[2 * e + 1]); b[e] = pk2(s[8 + 2 * e], s[8 + 2 * e + 1]); }
  p0 = __builtin_bit_cast(bf16x8, a); p1 = __builtin_bit_cast(bf16x8, b);
}
DI float xhalf_max(float m) { return fmaxf(m, __shfl_xor(m, 32)); }

DI unsigned lds_ld(LDSP char* p) { return __hip_atomic_load((LDSP unsigned*)p, __ATOMIC_RELAXED, __HIP_MEMORY_SCOPE_WORKGROUP); }
DI void lds_inc(unsigned addr) {
  unsigned va, v1; unsigned long long sv;
  asm volatile("v_mov_b32 %0, %3\n\tv_mov_b32 %1, 1\n\ts_mov_b64 %2, exec\n\ts_mov_b64 exec, 1\n\t"
               "ds_add_u32 %0, %1\n\ts_mov_b64 exec, %2"
               : "=&v"(va), "=&v"(v1), "=&s"(sv) : "s"(addr) : "memory");
}
DI void lds_wait(unsigned addr, unsigned need) {
  unsigned va, vv, sv;
  asm volatile("v_mov_b32 %0, %3\n"
               "LW%=:\n\tds_read_b32 %1, %0\n\ts_waitcnt lgkmcnt(0)\n\tv_readfirstlane_b32 %2, %1\n\ts_nop 3\n\t"
               "s_cmp_ge_u32 %2, %4\n\ts_cbranch_scc1 LD%=\n\ts_sleep 1\n\ts_branch LW%=\n"
               "LD%=:"
               : "=&v"(va), "=&v"(vv), "=&s"(sv) : "s"(addr), "s"(need) : "memory", "scc");
}

DI void attn_fallback(const char* kvh, bf16x8 q0, bf16x4 q1, int lane, f32x16& o_out) {
  const int r = lane & 31, hh = lane >> 5, rv = r < 21 ? r : 21;
  f32x16 o, zero;
#pragma unroll
  for (int e = 0; e < 16; ++e) { o[e] = 0.f; zero[e] = 0.f; }
  float m = -INFINITY;
  for (int t = 0; t < 64; ++t) {
    const char* st = kvh + (size_t)(t >> 3) * KV_STAGE;
    const int tt = t & 7;
    bf16x8 kf0 = *(const bf16x8*)(st + tt * KT + lane * 16);
    bf16x4 kf1 = *(const bf16x4*)(st + tt * KT + 1024 + r * 8);
    bf16x8 vf0 = *(const bf16x8*)(st + KV_VOFF + tt * VT + hh * VH + rv * 16);
    bf16x8 vf1 = *(const bf16x8*)(st + KV_VOFF + tt * VT + VS + hh * VH + rv * 16);
    f32x16 s = MFMA32(kf0, q0, zero);
    s = MFMA32K8(kf1, q1, s);
    float tm = s[0];
#pragma unroll
    for (int e = 1; e < 16; ++e) tm = fmaxf(tm, s[e]);
    tm = xhalf_max(tm);
    const float mn = fmaxf(m, tm);
    const float alpha = __builtin_amdgcn_exp2f(m - mn);
    m = mn;
#pragma unroll
    for (int e = 0; e < 16; ++e) { o[e] *= alpha; s[e] = __builtin_amdgcn_exp2f(s[e] - mn); }
    bf16x8 p0, p1;
    pack_p(s, p0, p1);
    o = MFMA32(vf0, p0, o);
    o = MFMA32(vf1, p1, o);
  }
  o_out = o;
}

DI bool frag_ready(const bf16x8& f) {
  const u32x4 w = __builtin_bit_cast(u32x4, f);
  unsigned m = 0xffffu;
#pragma unroll
  for (int e = 0; e < 4; ++e) { const unsigned t = ~w[e]; m = min(m, min(t & 0xffffu, t >> 16)); }
  return __all(m != 0u);
}
template <int NPRE, int NL>
DI void oproj(rsrc_t ro, rsrc_t rw, unsigned voff, const bf16x8 (&pre)[10], LDSP char* lds, unsigned lds0, int wid,
              LDSP char* pb TKARG) {
  constexpr int NT = NPRE + NL;
  bf16x8 bfr[10][NL ? NL : 1];
#pragma unroll
  for (int k = 0; k < 10; ++k)
#pragma unroll
    for (int c = 0; c < NL; ++c) bfr[k][c] = bload(rw, voff, k * 20480 + (NPRE + c) * 1024);
  __builtin_amdgcn_sched_barrier(0);
  TK(11);
  lds_wait(lds0 + K2_FLAGS + 32 + 7 * 4, 8u);
  TK(12);
  {
    unsigned pend = wid < 4 ? 0xfu : 0x7u;
    bf16x8 of[4];
    for (int spin = 0; pend != 0u && spin < (1 << 18); ++spin) {
#pragma unroll
      for (int i = 0; i < 4; ++i)
        if ((pend >> i) & 1u) of[i] = __builtin_bit_cast(bf16x8, __builtin_amdgcn_raw_buffer_load_b128(ro, voff, (wid + 12 * i) * 1024, 16));
#pragma unroll
      for (int i = 0; i < 4; ++i)
        if (((pend >> i) & 1u) && frag_ready(of[i])) {
          pend &= ~(1u << i);
          *(LDSP bf16x8*)(lds + (wid + 12 * i) * 1024 + voff) = of[i];
        }
    }
  }
  lds_inc(lds0 + K2_FLAGS + 72);
  lds_wait(lds0 + K2_FLAGS + 72, 12u);
  TK(13);
  const LDSP char* ab = lds + (wid & 3) * 10240 + voff;
  f32x16 acc[NT];
#pragma unroll
  for (int c = 0; c < NT; ++c)
#pragma unroll
    for (int e = 0; e < 16; ++e) acc[c][e] = 0.f;
#pragma unroll
  for (int k = 0; k < 10; ++k) {
    const bf16x8 af = *(const LDSP bf16x8*)(ab + k * 1024);
    if (NPRE) acc[0] = MFMA32(pre[k], af, acc[0]);
#pragma unroll
    for (int c = 0; c < NL; ++c) acc[NPRE + c] = MFMA32(bfr[k][c], af, acc[NPRE + c]);
  }
#pragma unroll
  for (int c = 0; c < NT; ++c)
#pragma unroll
    for (int g = 0; g < 4; ++g) {
      f32x4 v = {acc[c][4 * g], acc[c][4 * g + 1], acc[c][4 * g + 2], acc[c][4 * g + 3]};
      *(LDSP f32x4*)(pb + c * 128 + g * 32) = v;
    }
}

__global__ __launch_bounds__(768) void k2_kernel(char* __restrict__ ws, u16* __restrict__ out) {
  __shared__ __attribute__((aligned(16))) char lds_raw[K2_LDS];
  LDSP char* lds = (LDSP char*)lds_raw;
  const unsigned lds0 = (unsigned)(uintptr_t)lds;
  const int bid = blockIdx.x, xcd = bid & 7, idx = bid >> 3;
  const int tid = threadIdx.x, lane = tid & 63, wid = __builtin_amdgcn_readfirstlane(tid >> 6);
  const int r = lane & 31, hh = lane >> 5;
  STAMP(1, 0);
  if (wid < 8) {
    const int head = xcd * 4 + (idx >> 3), qt = idx & 7;
    const char* kvh = ws + KV_OFF + (size_t)head * 8 * KV_STAGE;
    const int rt = qt * 8 + wid;
    const char* qsrc = ws + QF_OFF + (size_t)(head * 64 + rt) * 1280;
    const bf16x8 q0 = *(const bf16x8*)(qsrc + lane * 16);
    bf16x4 q1 = *(const bf16x4*)(qsrc + 1024 + r * 8);
    const int jrt = xcd * 8 + (idx >> 2), cg = idx & 3, kq = wid & 3, tg = wid >> 2;
    const rsrc_t rw = mk_rsrc(ws + WOF_OFF + ((size_t)kq * 10 * 20 + cg * 5 + tg * 2) * 1024);
    if (hh) q1 = bf16x4{0, 0, 0, 0};
    const int rv = r < 21 ? r : 21;
    const int koff0 = lane * 16, koff1 = 1024 + (hh ? 256 : r * 8), voff = KV_VOFF + hh * VH + rv * 16;
    f32x16 o, zero;
#pragma unroll
    for (int e = 0; e < 16; ++e) { o[e] = 0.f; zero[e] = 0.f; }
    if (tid < 32) *(LDSP unsigned*)(lds + K2_FLAGS + tid * 4) = 0u;
    BARRIER();
    TK(1);
    bf16x8 wpre[10];
#pragma unroll
    for (int k = 0; k < 10; ++k) wpre[k] = bf16x8{0, 0, 0, 0, 0, 0, 0, 0};
    bf16x8 kf0 = *(const LDSP bf16x8*)(lds + koff0);
    bf16x4 kf1 = *(const LDSP bf16x4*)(lds + koff1);
    {
      f32x16 s = MFMA32(kf0, q0, zero);
      s = MFMA32K8(kf1, q1, s);
      float tm = s[0];
#pragma unroll
      for (int e = 1; e < 16; ++e) tm = fmaxf(tm, s[e]);
      tm = xhalf_max(tm);
      if (hh) q1[0] = (short)(pk2(-tm, 0.f) & 0xffffu);
    }
    f32x16 sA = MFMA32(kf0, q0, zero), sB;
    sA = MFMA32K8(kf1, q1, sA);
    kf0 = *(const LDSP bf16x8*)(lds + KT + koff0);
    kf1 = *(const LDSP bf16x4*)(lds + KT + koff1);
    u32x4 p0, p1 = {0u, 0u, 0u, 0u};
    bf16x8 vf0, vf1 = {0, 0, 0, 0, 0, 0, 0, 0};
#define SB() __builtin_amdgcn_sched_barrier(0)
#define PIN(x) asm volatile("" : "+v"(x))
#define EX(S, i) S[i] = __builtin_amdgcn_exp2f(S[i])
#define TILE(SC, SN, VP, KP)                                                                   \
  do {                                                                                         \
    EX(SC, 0); EX(SC, 1); SB();                                                                \
    o = MFMA32(vf1, __builtin_bit_cast(bf16x8, p1), o); SB();                                  \
    vf0 = *(const LDSP bf16x8*)((VP)); vf1 = *(const LDSP bf16x8*)((VP) + VS);                \
    EX(SC, 2); EX(SC, 3); p0[0] = pk2(SC[0], SC[1]); p0[1] = pk2(SC[2], SC[3]);                \
    EX(SC, 4); EX(SC, 5); SB();                                                                \
    SN = MFMA32(kf0, q0, zero); SB();                                                          \
    EX(SC, 6); EX(SC, 7); p0[2] = pk2(SC[4], SC[5]); p0[3] = pk2(SC[6], SC[7]);                \
    EX(SC, 8); EX(SC, 9); SB();                                                                \
    SN = MFMA32K8(kf1, q1, SN); SB();                                                          \
    kf0 = *(const LDSP bf16x8*)((KP) + koff0); kf1 = *(const LDSP bf16x4*)((KP) + koff1);      \
    EX(SC, 10); EX(SC, 11); p1[0] = pk2(SC[8], SC[9]); p1[1] = pk2(SC[10], SC[11]);            \
    EX(SC, 12); EX(SC, 13); SB();                                                              \
    o = MFMA32(vf0, __builtin_bit_cast(bf16x8, p0), o); SB();                                  \
    EX(SC, 14); EX(SC, 15); p1[2] = pk2(SC[12], SC[13]); p1[3] = pk2(SC[14], SC[15]); SB();    \
  } while (0)
    int slot = 0;
    TK(8);
    for (int stage = 0; stage < 8; ++stage) {
      const int nslot = slot == 2 ? 0 : slot + 1;
      const LDSP char* cb = lds + slot * KV_STAGE;
      const LDSP char* nb = lds + nslot * KV_STAGE;
      const LDSP char* vb = cb + voff;
      if (stage == 2) {
#pragma unroll
        for (int k = 0; k < 10; ++k) wpre[k] = bload(rw, lane * 16, k * 20480);
        SB();
      }
      TILE(sA, sB, vb + 0 * VT, cb + 2 * KT);
      TILE(sB, sA, vb + 1 * VT, cb + 3 * KT);
      TILE(sA, sB, vb + 2 * VT, cb + 4 * KT);
      TILE(sB, sA, vb + 3 * VT, cb + 5 * KT);
      TILE(sA, sB, vb + 4 * VT, cb + 6 * KT);
      const unsigned full = lds_ld(lds + K2_FLAGS + ((stage + 1) & 7) * 4);
      SB();
      TILE(sB, sA, vb + 5 * VT, cb + 7 * KT);
      if (stage == 0) TK(2); if (stage == 3) TK(3); if (stage == 6) TK(4); if (stage == 7) TK(5);
      if (stage < 7 && (unsigned)__builtin_amdgcn_readfirstlane(full) < 4u) lds_wait(lds0 + K2_FLAGS + (stage + 1) * 4, 4u);
      asm volatile("" ::: "memory");
      TILE(sA, sB, vb + 6 * VT, nb + 0 * KT);
      TILE(sB, sA, vb + 7 * VT, nb + 1 * KT);
      lds_inc(lds0 + K2_FLAGS + 32 + stage * 4);
      slot = nslot;
    }
    o = MFMA32(vf1, __builtin_bit_cast(bf16x8, p1), o);
    TK(9);
    {
      float l = __shfl(o[8], 32 + r);
      float amax = 0.f;
#pragma unroll
      for (int e = 0; e < 12; ++e) amax = fmaxf(amax, fabsf(o[e]));
      bool bad = !(l <= 1.0e36f) || !(l >= 8.7e-19f) || !(amax <= 3.0e38f);
#pragma unroll
      for (int e = 0; e < 12; ++e) bad = bad || (o[e] != o[e]);
      if (__any(bad)) { if (hh) q1[0] = 0; attn_fallback(kvh, q0, q1, lane, o); }
    }
    const float l = __shfl(o[8], 32 + r);
    const float inv = 1.f / l;
    const rsrc_t rws = mk_rsrc(ws);
    const unsigned odst = (unsigned)OF_OFF + rt * 40 * 1024;
#pragma unroll
    for (int e = 0; e < 12; ++e) {
      const float v = o[e] * inv;
      o[e] = (v != v) ? __uint_as_float(0x7fc00000u) : v;
    }
    u32x4 om;
#pragma unroll
    for (int e = 0; e < 4; ++e) om[e] = pk2(o[2 * e], o[2 * e + 1]);
    __builtin_amdgcn_raw_buffer_store_b128(om, rws, odst + head * 1024 + lane * 16, 0, 16);
    if (hh == 0) {
      u32x2 orr;
      orr[0] = pk2(o[8], o[9]);
      orr[1] = pk2(o[10], o[11]);
      __builtin_amdgcn_raw_buffer_store_b64(orr, rws, odst + (32 + (head >> 2)) * 1024 + (((head >> 1) & 1) * 32 + r) * 16 + (head & 1) * 8, 0, 16);
    }
    TK(10);
    const rsrc_t ro = mk_rsrc(ws + OF_OFF + (size_t)jrt * 40 * 1024);
    LDSP char* pb = lds + K2_RING + kq * PART_SZ + r * PART_ROW + hh * 16 + tg * 2 * 128;
    if (tg == 0) oproj<1, 1>(ro, rw, lane * 16, wpre, lds, lds0, wid, pb TKPASS);
    else oproj<1, 0>(ro, rw, lane * 16, wpre, lds, lds0, wid, pb TKPASS);
  } else {
#define GPRIO 0
#define GSLEEP 0
    __builtin_amdgcn_s_setprio(GPRIO);
    const int kq = wid - 8;
    const int rt = xcd * 8 + (idx >> 2), cg = idx & 3;
    const rsrc_t ra = mk_rsrc(ws + HF_OFF + ((size_t)rt * 108 + kq * 27) * 1024);
    const rsrc_t rb = mk_rsrc(ws + W2F_OFF + ((size_t)kq * 27 * 20 + cg * 5) * 1024);
    const unsigned voff = lane * 16;
    const int head = xcd * 4 + (idx >> 3);
    const char* dsrc = ws + KV_OFF + (size_t)head * 8 * KV_STAGE + kq * 1024;
    auto dma = [&](int stage) {
      const char* s = dsrc + (size_t)stage * KV_STAGE;
      const unsigned d = lds0 + (stage % 3) * KV_STAGE + kq * 1024;
#pragma unroll
      for (int i = 0; i < 5; ++i) glds16s(s + i * 4096, voff, d + i * 4096);
      glds16s(s + 20480 - (kq & 2) * 1024, voff, d + 20480 - (kq & 2) * 1024);
    };
    dma(0);
    dma(1);
    f32x16 acc[5];
#pragma unroll
    for (int c = 0; c < 5; ++c)
#pragma unroll
      for (int e = 0; e < 16; ++e) acc[c][e] = 0.f;
    constexpr int PD = 3;
    bf16x8 af[PD], bfr[PD][5];
#pragma unroll
    for (int j = 0; j < PD; ++j) {
      af[j] = bload(ra, voff, j * 1024);
#pragma unroll
      for (int c = 0; c < 5; ++c) bfr[j][c] = bload(rb, voff, j * 20480 + c * 1024);
    }
    __builtin_amdgcn_sched_barrier(0);
    WAITV(24);
    BARRIER();
    __builtin_amdgcn_sched_barrier(0);
#pragma unroll
    for (int stage = 0; stage < 7; ++stage) {
#pragma unroll
      for (int i = 0; i < 4; ++i) {
        const int k = stage * 4 + i;
        if (k < 27) {
          const int j = k % PD;
#pragma unroll
          for (int c = 0; c < 5; ++c) {
            acc[c] = MFMA32(bfr[j][c], af[j], acc[c]);
          }
          __builtin_amdgcn_sched_barrier(0);
          if (k + PD < 27) {
            af[j] = bload(ra, voff, (k + PD) * 1024);
#pragma unroll
            for (int c = 0; c < 5; ++c) bfr[j][c] = bload(rb, voff, (k + PD) * 20480 + c * 1024);
          }
          __builtin_amdgcn_sched_barrier(0);
        }
        if (i == 1) {
          if (stage < 6) WAITV(12); else WAITV(0);
          lds_inc(lds0 + K2_FLAGS + (stage + 1) * 4);
          __builtin_amdgcn_sched_barrier(0);
        }
      }
      TK(2 + stage);
      if (stage + 2 < 8) {
        if (stage >= 1) lds_wait(lds0 + K2_FLAGS + 32 + (stage - 1) * 4, 8u);
        dma(stage + 2);
      }
      __builtin_amdgcn_sched_barrier(0);
    }
    {
      unsigned vz = voff;
      asm volatile("" : "+v"(vz));
      const int l2 = vz >> 4;
      LDSP char* pb = lds + K2_RING + kq * PART_SZ + (l2 & 31) * PART_ROW + (l2 >> 5) * 16;
#pragma unroll
      for (int c = 0; c < 5; ++c)
#pragma unroll
        for (int g = 0; g < 4; ++g) {
          f32x4 v = {acc[c][4 * g], acc[c][4 * g + 1], acc[c][4 * g + 2], acc[c][4 * g + 3]};
          *(LDSP f32x4*)(pb + c * 128 + g * 32) = v;
        }
      lds_inc(lds0 + K2_FLAGS + 64);
      lds_wait(lds0 + K2_FLAGS + 64, 4u);
      lds_wait(lds0 + K2_FLAGS + 32 + 5 * 4, 8u);
    }
    __builtin_amdgcn_s_setprio(3);
    unsigned vy = voff;
    asm volatile("" : "+v"(vy));
    for (int it = kq * 64 + (vy >> 4); it < 640; it += 256) {
      const int row = it / 20, c8 = it - row * 20;
      const LDSP char* pb = lds + K2_RING + row * PART_ROW + c8 * 32;
      f32x4 s0 = *(const LDSP f32x4*)(pb), s1 = *(const LDSP f32x4*)(pb + 16);
#pragma unroll
      for (int w = 1; w < 4; ++w) {
        s0 += *(const LDSP f32x4*)(pb + w * PART_SZ);
        s1 += *(const LDSP f32x4*)(pb + w * PART_SZ + 16);
      }
      LDSP char* qb = lds + 2 * KV_STAGE + row * PART_ROW + c8 * 32;
      *(LDSP f32x4*)(qb) = s0;
      *(LDSP f32x4*)(qb + 16) = s1;
    }
    TK(10);
    {
      const rsrc_t ro = mk_rsrc(ws + OF_OFF + (size_t)rt * 40 * 1024);
      const rsrc_t rw = mk_rsrc(ws + WOF_OFF + ((size_t)kq * 10 * 20 + cg * 5 + 3) * 1024);
      const int l2 = vy >> 4;
      LDSP char* pb = lds + K2_RING + kq * PART_SZ + (l2 & 31) * PART_ROW + (l2 >> 5) * 16 + 3 * 128;
      bf16x8 none[10];
      oproj<0, 2>(ro, rw, vy, none, lds, lds0, wid, pb TKPASS);
    }
  }
  BARRIER();
  TK(14);
  {
    const int jrt = xcd * 8 + (idx >> 2), cg = idx & 3;
    if (tid < 640) {
      const int row = tid / 20, c8 = tid - row * 20;
      const float g = *(const float*)(ws + G_OFF + (size_t)(jrt * 32 + row) * 4);
      const LDSP char* p = lds + K2_RING + row * PART_ROW + c8 * 32;
      f32x4 s0 = *(const LDSP f32x4*)(p), s1 = *(const LDSP f32x4*)(p + 16);
#pragma unroll
      for (int w = 1; w < 4; ++w) {
        s0 += *(const LDSP f32x4*)(p + w * PART_SZ);
        s1 += *(const LDSP f32x4*)(p + w * PART_SZ + 16);
      }
      const LDSP char* qb = lds + 2 * KV_STAGE + row * PART_ROW + c8 * 32;
      const f32x4 b0 = *(const LDSP f32x4*)(qb), b1 = *(const LDSP f32x4*)(qb + 16);
      const float h = 1.f - g;
      u32x4 o;
      o[0] = pk2(g * s0[0] + h * b0[0], g * s0[1] + h * b0[1]);
      o[1] = pk2(g * s0[2] + h * b0[2], g * s0[3] + h * b0[3]);
      o[2] = pk2(g * s1[0] + h * b1[0], g * s1[1] + h * b1[1]);
      o[3] = pk2(g * s1[2] + h * b1[2], g * s1[3] + h * b1[3]);
      *(u32x4*)(out + (size_t)(jrt * 32 + row) * 640 + cg * 160 + c8 * 8) = o;
    }
  }
  STAMP(1, 15);
}

extern "C" void kernel_launch(void* const* d_in, const int* in_sizes, int n_in, void* d_out, int out_size, void* d_ws,
                              size_t ws_size, hipStream_t stream) {
  const u16* x = (const u16*)d_in[0];
  const u16* wqkv = (const u16*)d_in[1];
  const u16* wo = (const u16*)d_in[2];
  const u16* w1 = (const u16*)d_in[4];
  const u16* w2 = (const u16*)d_in[5];
  const u16* gw = (const u16*)d_in[6];
  char* ws = (char*)d_ws;
  k1_kernel<<<256, 768, 0, stream>>>(x, wqkv, w1, w2, wo, gw, ws);
  k2_kernel<<<256, 768, 0, stream>>>(ws, (u16*)d_out);
}
```

```cpp
#include <hip/hip_runtime.h>
#include <hip/hip_bf16.h>
#include <stdint.h>

typedef __attribute__((ext_vector_type(8))) short bf16x8;
typedef __attribute__((ext_vector_type(4))) short bf16x4;
typedef __attribute__((ext_vector_type(16))) float f32x16;
typedef __attribute__((ext_vector_type(4))) float f32x4;
typedef __attribute__((ext_vector_type(2))) float f32x2;
typedef __attribute__((ext_vector_type(4))) unsigned u32x4;
typedef __attribute__((ext_vector_type(2))) unsigned u32x2;
typedef __attribute__((ext_vector_type(2))) __bf16 bf16x2_t;
typedef unsigned short u16;
#define LDSP __attribute__((address_space(3)))
#define DI __device__ __forceinline__

constexpr int SEQ = 2048, DM = 640, NH = 32, DFF = 1728;
constexpr int KT = 256, VT = 1408, VS = 704, VH = 352;
constexpr int KV_VOFF = 6 * 2048;
constexpr int VSTAGE = 8 * VT;
constexpr int KV_STAGE = KV_VOFF + VSTAGE;
constexpr size_t QK_OFF = 0;
constexpr size_t CB_OFF = QK_OFF + (size_t)320 * 16384;
constexpr size_t V_OFF = CB_OFF + 2048;
constexpr size_t HF_OFF = V_OFF + (size_t)32 * 8 * VSTAGE;
constexpr size_t W2F_OFF = HF_OFF + (size_t)64 * 108 * 1024;
constexpr size_t WOF_OFF = W2F_OFF + (size_t)108 * 20 * 1024;
constexpr size_t OF_OFF = WOF_OFF + (size_t)40 * 20 * 1024;
constexpr size_t G_OFF = OF_OFF + (size_t)64 * 40 * 1024;

#define TK(i)
#define TKARG
#define TKPASS
#define STAMP(kid, slot)
#define DIAGWS
#define MFMA32(a, b, c) __builtin_amdgcn_mfma_f32_32x32x16_bf16((a), (b), (c), 0, 0, 0)
#define MFMA32K8(a, b, c) __builtin_amdgcn_mfma_f32_32x32x8bf16_1k((a), (b), (c), 0, 0, 0)

DI unsigned pk2(float lo, float hi) {
  f32x2 v = {lo, hi};
  bf16x2_t r = __builtin_convertvector(v, bf16x2_t);
  return __builtin_bit_cast(unsigned, r);
}
DI float bf_lo(unsigned u) { return __uint_as_float(u << 16); }
DI float bf_hi(unsigned u) { return __uint_as_float(u & 0xffff0000u); }

DI void glds16(const void* g, unsigned lds_dst) {
  unsigned sv;
  asm volatile("s_mov_b32 %0, m0\n\ts_mov_b32 m0, %2\n\ts_nop 0\n\tglobal_load_lds_dwordx4 %1, off\n\ts_mov_b32 m0, %0"
               : "=&s"(sv) : "v"(g), "s"(lds_dst) : "memory");
}
DI void glds16s(const void* sbase, unsigned voff, unsigned lds_dst) {
  unsigned sv;
  asm volatile("s_mov_b32 %0, m0\n\ts_mov_b32 m0, %3\n\ts_nop 0\n\tglobal_load_lds_dwordx4 %1, %2\n\ts_mov_b32 m0, %0"
               : "=&s"(sv) : "v"(voff), "s"(sbase), "s"(lds_dst) : "memory");
}
DI void glds16m(const void* sbase, unsigned voff, unsigned lds_dst) {
  asm volatile("s_mov_b32 m0, %2\n\ts_nop 0\n\tglobal_load_lds_dwordx4 %0, %1" :: "v"(voff), "s"(sbase), "s"(lds_dst) : "memory");
}
typedef __amdgpu_buffer_rsrc_t rsrc_t;
DI rsrc_t mk_rsrc(const void* p) { return __builtin_amdgcn_make_buffer_rsrc((void*)p, 0, 0x7fffffff, 0x00020000); }
DI bf16x8 bload(rsrc_t r, unsigned voff, unsigned soff) {
  return __builtin_bit_cast(bf16x8, __builtin_amdgcn_raw_buffer_load_b128(r, voff, soff, 0));
}
#define STAUX 0
DI void st16(rsrc_t r, unsigned off, u32x4 v) { __builtin_amdgcn_raw_buffer_store_b128(v, r, off, 0, STAUX); }
#define STAUXH 0
DI void st16h(rsrc_t r, unsigned off, u32x4 v) { __builtin_amdgcn_raw_buffer_store_b128(v, r, off, 0, STAUXH); }
DI void st8(rsrc_t r, unsigned off, u32x2 v) { __builtin_amdgcn_raw_buffer_store_b64(v, r, off, 0, STAUX); }
DI bf16x4 tr_read(const LDSP char* p) {
  return __builtin_amdgcn_ds_read_tr16_b64_v4i16((LDSP bf16x4*)p);
}
#define WAITV(n) asm volatile("s_waitcnt vmcnt(" #n ")" ::: "memory")
#define BARRIER() asm volatile("s_waitcnt lgkmcnt(0)\n\ts_barrier" ::: "memory")

constexpr int K1_SLOT = 49152;

template <bool TR>
DI void k1_mainloop(f32x16 (&acc)[2][2], LDSP char* lds, const int (&aoff)[4], const int (&boff)[2]
                    ) {
  bf16x8 fa[2][2], fb[2][2];
  auto rd = [&](int buf, int kt, int ks) {
    const LDSP char* sa = lds + (kt % 3) * K1_SLOT;
    const LDSP char* sb = sa + 32768;
#pragma unroll
    for (int nt = 0; nt < 2; ++nt) {
      bf16x4 lo = tr_read(sb + boff[nt] + ks * 4096);
      bf16x4 hi = tr_read(sb + boff[nt] + ks * 4096 + 1024);
      fb[buf][nt] = __builtin_shufflevector(lo, hi, 0, 1, 2, 3, 4, 5, 6, 7);
    }
#pragma unroll
    for (int mt = 0; mt < 2; ++mt) fa[buf][mt] = *(const LDSP bf16x8*)(sa + aoff[ks] + mt * 4096);
  };
  BARRIER();
  rd(0, 0, 0);
  __builtin_amdgcn_sched_barrier(0);
#pragma unroll
  for (int kt = 0; kt < 10; ++kt) {
#pragma unroll
    for (int ks = 0; ks < 4; ++ks) {
      const int cb = ks & 1;
      if (ks < 3) rd(cb ^ 1, kt, ks + 1);
      else if (kt < 9) {
        BARRIER();
        rd(cb ^ 1, kt + 1, 0);
      }
      __builtin_amdgcn_sched_barrier(0);
#pragma unroll
      for (int mt = 0; mt < 2; ++mt)
#pragma unroll
        for (int nt = 0; nt < 2; ++nt)
          acc[mt][nt] = TR ? MFMA32(fb[cb][nt], fa[cb][mt], acc[mt][nt]) : MFMA32(fa[cb][mt], fb[cb][nt], acc[mt][nt]);
      __builtin_amdgcn_sched_barrier(0);
    }
  }
}

DI void k1_loader(int lw, int lane, unsigned lds0, const u16* xa, const u16* wb, int ldw, bool half_tile) {
  unsigned aoffs[2], boffs;
#pragma unroll
  for (int par = 0; par < 2; ++par) aoffs[par] = (lane >> 3) * 1280 + (((lane & 7) ^ ((4 * par + (lane >> 4)) & 7)) * 16);
  {
    int c = (lane & 15) ^ (((lane >> 4) & 3) << 2);
    if (half_tile) c &= 7;
    boffs = (lane >> 4) * ldw * 2 + c * 16;
  }
  const u16* xp = xa + (size_t)lw * 64 * 640;
  const u16* wp = wb + (size_t)lw * 16 * ldw;
  unsigned sl = lds0;
  auto issue = [&]() {
#pragma unroll 1
    for (int i = 0; i < 4; ++i) {
      glds16m(xp + (size_t)i * 16 * 640, aoffs[0], sl + lw * 8192 + i * 2048);
      glds16m(xp + (size_t)i * 16 * 640 + 8 * 640, aoffs[1], sl + lw * 8192 + i * 2048 + 1024);
      glds16m(wp + (size_t)i * 4 * ldw, boffs, sl + 32768 + lw * 4096 + i * 1024);
    }
    xp += 64;
    wp += (size_t)64 * ldw;
    sl = (sl == lds0 + 2 * K1_SLOT) ? lds0 : sl + K1_SLOT;
  };
#pragma unroll 1
  for (int kt = -2; kt < 8; ++kt) {
    if (kt >= 0) { WAITV(12); BARRIER(); }
    issue();
  }
  WAITV(12); BARRIER();
  WAITV(0); BARRIER();
}

DI void k1_aux(int aux, LDSP char* lds, const u16* x, const u16* w2, const u16* wo, const u16* gw, char* ws) {
  const int tid = threadIdx.x, lane = tid & 63, wid = __builtin_amdgcn_readfirstlane(tid >> 6);
  const unsigned lds0 = (unsigned)(uintptr_t)lds;
  const rsrc_t rws = mk_rsrc(ws);
  {
    const u32x4 ones = {0x3f803f80u, 0x3f803f80u, 0x3f803f80u, 0x3f803f80u}, zeros = {0u, 0u, 0u, 0u};
    if (aux == 0 && tid < 128) st16(rws, (unsigned)CB_OFF + tid * 16, u32x4{0x00003f80u, 0u, 0x00003f80u, 0u});
    for (int i = aux * 768 + tid; i < 2048 * 8; i += 24 * 768)
      st16(rws, (unsigned)V_OFF + (i >> 3) * VT + ((i & 7) >> 1) * VH + (20 + (i & 1)) * 16, (i & 1) ? zeros : ones);
  }
  {
    const u32x4 ff = {0xffffffffu, 0xffffffffu, 0xffffffffu, 0xffffffffu};
    for (int i = aux * 768 + tid; i < 64 * 40 * 64; i += 24 * 768) st16(rws, (unsigned)OF_OFF + i * 16, ff);
  }
  constexpr int SLAB = 16 * 1344;
  for (int pc = wid; pc < 147; pc += 12) {
    const int sl = pc / 21, pp = pc - sl * 21;
    int ksg = aux + sl * 24;
    if (ksg >= 148) ksg = 147;
    const int i = pp * 64 + lane, rho = i / 84;
    int c = i - rho * 84;
    if (c > 79) c = 79;
    const bool isw2 = ksg < 108;
    const int ks = isw2 ? ksg : ksg - 108;
    int row;
    if (isw2) row = 16 * ks + rho;
    else if (ks < 32) row = 20 * ks + rho;
    else row = 20 * (4 * (ks - 32) + 2 * ((rho >> 2) & 1) + (rho >> 3)) + 16 + (rho & 3);
    glds16((isw2 ? w2 : wo) + (size_t)row * 640 + c * 8, lds0 + pc * 1024);
  }
  const int wv = aux * 12 + wid, sub = lane >> 4, l16 = lane & 15;
  u32x4 g4[5], x4[2][5];
#pragma unroll
  for (int c = 0; c < 5; ++c) g4[c] = *(const u32x4*)(gw + (c * 16 + l16) * 8);
#pragma unroll
  for (int i = 0; i < 2; ++i) {
    int row = wv * 4 + i * 1152 + sub;
    if (row >= SEQ) row = SEQ - 1;
#pragma unroll
    for (int c = 0; c < 5; ++c) x4[i][c] = *(const u32x4*)(x + (size_t)row * 640 + (c * 16 + l16) * 8);
  }
  float* gout = (float*)(ws + G_OFF);
#pragma unroll
  for (int i = 0; i < 2; ++i) {
    const int row = wv * 4 + i * 1152 + sub;
    float s = 0.f;
#pragma unroll
    for (int c = 0; c < 5; ++c)
#pragma unroll
      for (int e = 0; e < 4; ++e) { s += bf_lo(x4[i][c][e]) * bf_lo(g4[c][e]); s += bf_hi(x4[i][c][e]) * bf_hi(g4[c][e]); }
    s += __shfl_xor(s, 1); s += __shfl_xor(s, 2); s += __shfl_xor(s, 4); s += __shfl_xor(s, 8);
    if (l16 == 0 && row < SEQ) gout[row] = 1.f / (1.f + __expf(-s));
  }
  WAITV(0);
  BARRIER();
  const int toff = (4 * (lane >> 5) + ((lane & 15) >> 2)) * 1344 + ((lane >> 4) & 1) * 32 + (lane & 3) * 8;
  for (int f = wid; f < 140; f += 12) {
    const int sl = f / 20, ct = f - sl * 20;
    const int ksg = aux + sl * 24;
    if (ksg >= 148) break;
    const LDSP char* pb = lds + sl * SLAB + toff + ct * 64;
    bf16x4 lo = tr_read(pb), hi = tr_read(pb + 8 * 1344);
    bf16x8 v = __builtin_shufflevector(lo, hi, 0, 1, 2, 3, 4, 5, 6, 7);
    st16(rws, (unsigned)W2F_OFF + (ksg * 20 + ct) * 1024 + lane * 16, __builtin_bit_cast(u32x4, v));
  }
}

__global__ __launch_bounds__(768) void k1_kernel(const u16* __restrict__ x, const u16* __restrict__ wqkv,
                                                 const u16* __restrict__ w1, const u16* __restrict__ w2,
                                                 const u16* __restrict__ wo, const u16* __restrict__ gw,
                                                 char* __restrict__ ws) {
  __shared__ __attribute__((aligned(16))) char lds_raw[147 * 1024];
  const int bid = blockIdx.x, xcd = bid & 7, idx = bid >> 3;
  STAMP(0, 0);
  if (idx >= 29) { k1_aux(xcd * 3 + (idx - 29), (LDSP char*)lds_raw, x, w2, wo, gw, ws); STAMP(0, 2); return; }
  const int ii = (xcd & 3) * 29 + idx, nti = ii >> 2, mti = (xcd >> 2) * 4 + (ii & 3);
  const int tid = threadIdx.x, lane = tid & 63, wid = __builtin_amdgcn_readfirstlane(tid >> 6);
  const int r = lane & 31, hh = lane >> 5, wm = wid >> 1, wn = wid & 1;
  const int m0 = mti * 256;
  const bool isq = nti < 15;
  const u16* W = isq ? wqkv : w1;
  const int ldw = isq ? 1920 : DFF;
  const int n0 = isq ? nti * 128 : (nti - 15) * 128;
  const bool half_tile = (nti == 28);
  LDSP char* lds = (LDSP char*)lds_raw;
  const unsigned lds0 = (unsigned)(uintptr_t)lds;

  if (wid >= 8) { k1_loader(wid - 8, lane, lds0, x + (size_t)m0 * 640, W + n0, ldw, half_tile); return; }
  int aoff[4], boff[2];
#pragma unroll
  for (int ks = 0; ks < 4; ++ks) aoff[ks] = (wm * 64 + r) * 128 + (((2 * ks + hh) ^ ((r >> 1) & 7)) * 16);
  {
    const int q = (lane & 15) >> 2, p = lane & 3, blk = (lane >> 4) & 1;
#pragma unroll
    for (int nt = 0; nt < 2; ++nt)
      boff[nt] = (8 * hh + q) * 256 + ((((wn * 2 + nt) ^ q) * 4 + 2 * blk + (p >> 1)) * 16) + 8 * (p & 1);
  }
  f32x16 acc[2][2];
#pragma unroll
  for (int mt = 0; mt < 2; ++mt)
#pragma unroll
    for (int nt = 0; nt < 2; ++nt)
#pragma unroll
      for (int e = 0; e < 16; ++e) acc[mt][nt][e] = 0.f;

  const bool isv = (nti >= 10 && nti < 15);
  const rsrc_t rws = mk_rsrc(ws);
  const int rb0 = m0 + wm * 64;
  unsigned dsto[2] = {0u, 0u};
  float sc = 1.f;
  if (isv) {
#pragma unroll
    for (int nt = 0; nt < 2; ++nt) {
      const int col = (nti - 10) * 128 + wn * 64 + nt * 32 + r, head = col / 20, d = col - head * 20;
      dsto[nt] = (unsigned)V_OFF + (head * 64 + (rb0 >> 5)) * VT + hh * VH + d * 16;
    }
  } else if (nti < 10) {
    if (nti < 5) sc = 0.22360679774997896f * 1.4426950408889634f;
    dsto[0] = (unsigned)QK_OFF + (nti * 32 + wn * 16 + hh) * 16384 + (rb0 + r) * 8;
  } else {
    dsto[0] = (unsigned)HF_OFF + ((rb0 >> 5) * 108 + (((nti - 15) * 128 + wn * 64) >> 4)) * 1024 + lane * 16;
    if (half_tile && wn == 1) dsto[0] = 0x80000000u;
  }
  const unsigned da = dsto[0], db = dsto[1];
  if (isv) {
    k1_mainloop<false>(acc, lds, aoff, boff DIAGWS);
    STAMP(0, 1);
    {
#pragma unroll
      for (int nt = 0; nt < 2; ++nt)
#pragma unroll
        for (int mt = 0; mt < 2; ++mt)
#pragma unroll
          for (int s = 0; s < 2; ++s) {
            u32x4 o;
#pragma unroll
            for (int e = 0; e < 4; ++e) o[e] = pk2(acc[mt][nt][8 * s + 2 * e], acc[mt][nt][8 * s + 2 * e + 1]);
            __builtin_amdgcn_raw_buffer_store_b128(o, rws, nt ? db : da, mt * VT + s * VS, 0);
          }
    }
  } else {
    k1_mainloop<true>(acc, lds, aoff, boff DIAGWS);
    STAMP(0, 1);
    {
      if (nti < 10) {
#pragma unroll
        for (int nt = 0; nt < 2; ++nt)
#pragma unroll
          for (int g = 0; g < 4; ++g)
#pragma unroll
            for (int mt = 0; mt < 2; ++mt) {
              u32x2 o;
              o[0] = pk2(acc[mt][nt][4 * g] * sc, acc[mt][nt][4 * g + 1] * sc);
              o[1] = pk2(acc[mt][nt][4 * g + 2] * sc, acc[mt][nt][4 * g + 3] * sc);
              __builtin_amdgcn_raw_buffer_store_b64(o, rws, da, (nt * 8 + g * 2) * 16384 + mt * 256, 0);
            }
      } else {
#pragma unroll
        for (int nt = 0; nt < 2; ++nt)
#pragma unroll
          for (int mt = 0; mt < 2; ++mt)
#pragma unroll
            for (int s = 0; s < 2; ++s) {
              u32x4 o;
#pragma unroll
              for (int e = 0; e < 4; ++e)
                o[e] = pk2(fmaxf(acc[mt][nt][8 * s + 2 * e], 0.f), fmaxf(acc[mt][nt][8 * s + 2 * e + 1], 0.f));
              __builtin_amdgcn_raw_buffer_store_b128(o, rws, da, mt * (108 * 1024) + (nt * 2 + s) * 1024, 0);
            }
      }
    }
  }
  STAMP(0, 2);
}

constexpr int K2_RING = 3 * KV_STAGE;
constexpr int PART_ROW = 656;
constexpr int PART_SZ = 32 * PART_ROW;
constexpr int K2_FLAGS = K2_RING + 4 * PART_SZ;
constexpr int K2_LDS = K2_FLAGS + 128;

DI void pack_p(const f32x16& s, bf16x8& p0, bf16x8& p1) {
  u32x4 a, b;
#pragma unroll
  for (int e = 0; e < 4; ++e) { a[e] = pk2(s[2 * e], s[2 * e + 1]); b[e] = pk2(s[8 + 2 * e], s[8 + 2 * e + 1]); }
  p0 = __builtin_bit_cast(bf16x8, a); p1 = __builtin_bit_cast(bf16x8, b);
}
DI float xhalf_max(float m) { return fmaxf(m, __shfl_xor(m, 32)); }

DI unsigned lds_ld(LDSP char* p) { return __hip_atomic_load((LDSP unsigned*)p, __ATOMIC_RELAXED, __HIP_MEMORY_SCOPE_WORKGROUP); }
DI void lds_inc(unsigned addr) {
  unsigned va, v1; unsigned long long sv;
  asm volatile("v_mov_b32 %0, %3\n\tv_mov_b32 %1, 1\n\ts_mov_b64 %2, exec\n\ts_mov_b64 exec, 1\n\t"
               "ds_add_u32 %0, %1\n\ts_mov_b64 exec, %2"
               : "=&v"(va), "=&v"(v1), "=&s"(sv) : "s"(addr) : "memory");
}
DI void lds_wait(unsigned addr, unsigned need) {
  unsigned va, vv, sv;
  asm volatile("v_mov_b32 %0, %3\n"
               "LW%=:\n\tds_read_b32 %1, %0\n\ts_waitcnt lgkmcnt(0)\n\tv_readfirstlane_b32 %2, %1\n\ts_nop 3\n\t"
               "s_cmp_ge_u32 %2, %4\n\ts_cbranch_scc1 LD%=\n\ts_sleep 1\n\ts_branch LW%=\n"
               "LD%=:"
               : "=&v"(va), "=&v"(vv), "=&s"(sv) : "s"(addr), "s"(need) : "memory", "scc");
}

DI void attn_fallback(const char* kh, const char* vh, bf16x8 q0, bf16x4 q1, int lane, f32x16& o_out) {
  const int r = lane & 31, hh = lane >> 5, rv = r < 21 ? r : 21;
  f32x16 o, zero;
#pragma unroll
  for (int e = 0; e < 16; ++e) { o[e] = 0.f; zero[e] = 0.f; }
  float m = -INFINITY;
  for (int t = 0; t < 64; ++t) {
    const char* kp = kh + (t * 32 + r) * 8;
    const bf16x4 ka = *(const bf16x4*)(kp + (2 * hh) * 16384), kb = *(const bf16x4*)(kp + (2 * hh + 1) * 16384);
    const bf16x8 kf0 = __builtin_shufflevector(ka, kb, 0, 1, 2, 3, 4, 5, 6, 7);
    const bf16x4 kf1 = *(const bf16x4*)(kp + 4 * 16384);
    const bf16x8 vf0 = *(const bf16x8*)(vh + t * VT + hh * VH + rv * 16);
    const bf16x8 vf1 = *(const bf16x8*)(vh + t * VT + VS + hh * VH + rv * 16);
    f32x16 s = MFMA32(kf0, q0, zero);
    s = MFMA32K8(kf1, q1, s);
    float tm = s[0];
#pragma unroll
    for (int e = 1; e < 16; ++e) tm = fmaxf(tm, s[e]);
    tm = xhalf_max(tm);
    const float mn = fmaxf(m, tm);
    const float alpha = __builtin_amdgcn_exp2f(m - mn);
    m = mn;
#pragma unroll
    for (int e = 0; e < 16; ++e) { o[e] *= alpha; s[e] = __builtin_amdgcn_exp2f(s[e] - mn); }
    bf16x8 p0, p1;
    pack_p(s, p0, p1);
    o = MFMA32(vf0, p0, o);
    o = MFMA32(vf1, p1, o);
  }
  o_out = o;
}

DI bool frag_ready(const bf16x8& f) {
  const u32x4 w = __builtin_bit_cast(u32x4, f);
  unsigned m = 0xffffu;
#pragma unroll
  for (int e = 0; e < 4; ++e) { const unsigned t = ~w[e]; m = min(m, min(t & 0xffffu, t >> 16)); }
  return __all(m != 0u);
}
template <int NPRE, int NL, bool POLL>
DI void oproj(rsrc_t ro, rsrc_t rw, unsigned voff, const bf16x8 (&pre)[10], LDSP char* lds, unsigned lds0, int wid,
              LDSP char* pb, float gj TKARG) {
  constexpr int NT = NPRE + NL;
  bf16x8 bfr[10][NL ? NL : 1];
#pragma unroll
  for (int k = 0; k < 10; ++k)
#pragma unroll
    for (int c = 0; c < NL; ++c) bfr[k][c] = bload(rw, voff, k * 20480 + (NPRE + c) * 1024);
  __builtin_amdgcn_sched_barrier(0);
  TK(11);
  if (POLL) {
    lds_wait(lds0 + K2_FLAGS + 32 + 7 * 4, 8u);
    TK(12);
    unsigned pend = 0x1fu;
    bf16x8 of[5];
    for (int spin = 0; pend != 0u && spin < (1 << 18); ++spin) {
#pragma unroll
      for (int i = 0; i < 5; ++i)
        if ((pend >> i) & 1u) of[i] = __builtin_bit_cast(bf16x8, __builtin_amdgcn_raw_buffer_load_b128(ro, voff, (wid + 8 * i) * 1024, 16));
#pragma unroll
      for (int i = 0; i < 5; ++i)
        if (((pend >> i) & 1u) && frag_ready(of[i])) {
          pend &= ~(1u << i);
          *(LDSP bf16x8*)(lds + (wid + 8 * i) * 1024 + voff) = of[i];
        }
    }
  }
  lds_inc(lds0 + K2_FLAGS + 72);
  lds_wait(lds0 + K2_FLAGS + 72, 12u);
  TK(13);
  const LDSP char* ab = lds + (wid & 3) * 10240 + voff;
  f32x16 acc[NT];
#pragma unroll
  for (int c = 0; c < NT; ++c)
#pragma unroll
    for (int e = 0; e < 16; ++e) acc[c][e] = 0.f;
#pragma unroll
  for (int k = 0; k < 10; ++k) {
    const bf16x8 af = *(const LDSP bf16x8*)(ab + k * 1024);
    if (NPRE) acc[0] = MFMA32(pre[k], af, acc[0]);
#pragma unroll
    for (int c = 0; c < NL; ++c) acc[NPRE + c] = MFMA32(bfr[k][c], af, acc[NPRE + c]);
  }
#pragma unroll
  for (int c = 0; c < NT; ++c)
#pragma unroll
    for (int g = 0; g < 4; ++g) {
      f32x4 v = *(const LDSP f32x4*)(pb + c * 128 + g * 32);
#pragma unroll
      for (int e = 0; e < 4; ++e) v[e] += gj * acc[c][4 * g + e];
      *(LDSP f32x4*)(pb + c * 128 + g * 32) = v;
    }
}

__global__ __launch_bounds__(768) void k2_kernel(char* __restrict__ ws, u16* __restrict__ out) {
  __shared__ __attribute__((aligned(16))) char lds_raw[K2_LDS];
  LDSP char* lds = (LDSP char*)lds_raw;
  const unsigned lds0 = (unsigned)(uintptr_t)lds;
  const int bid = blockIdx.x, xcd = bid & 7, idx = bid >> 3;
  const int tid = threadIdx.x, lane = tid & 63, wid = __builtin_amdgcn_readfirstlane(tid >> 6);
  const int r = lane & 31, hh = lane >> 5;
  STAMP(1, 0);
  if (wid < 8) {
    const int head = xcd * 4 + (idx >> 3), qt = idx & 7;
    const int rt = qt * 8 + wid;
    const char* qsrc = ws + QK_OFF + (size_t)head * 5 * 16384 + (rt * 32 + r) * 8;
    const bf16x4 qa = *(const bf16x4*)(qsrc + (2 * hh) * 16384), qb = *(const bf16x4*)(qsrc + (2 * hh + 1) * 16384);
    const bf16x8 q0 = __builtin_shufflevector(qa, qb, 0, 1, 2, 3, 4, 5, 6, 7);
    bf16x4 q1 = *(const bf16x4*)(qsrc + 4 * 16384);
    const int jrt = xcd * 8 + (idx >> 2), cg = idx & 3, kq = wid & 3, tg = wid >> 2;
    const rsrc_t rw = mk_rsrc(ws + WOF_OFF + ((size_t)kq * 10 * 20 + cg * 5 + tg * 2) * 1024);
    if (hh) q1 = bf16x4{0, 0, 0, 0};
    const int rv = r < 21 ? r : 21;
    const int koff0 = hh * 4096 + r * 8, koff1 = (4 + hh) * 2048 + r * 8, voff = KV_VOFF + hh * VH + rv * 16;
#define KLD0(P) __builtin_shufflevector(*(const LDSP bf16x4*)((P) + koff0), *(const LDSP bf16x4*)((P) + koff0 + 2048), 0, 1, 2, 3, 4, 5, 6, 7)
    f32x16 o, zero;
#pragma unroll
    for (int e = 0; e < 16; ++e) { o[e] = 0.f; zero[e] = 0.f; }
    if (tid < 32) *(LDSP unsigned*)(lds + K2_FLAGS + tid * 4) = 0u;
    BARRIER();
    TK(1);
    bf16x8 wpre[10];
#pragma unroll
    for (int k = 0; k < 10; ++k) wpre[k] = bf16x8{0, 0, 0, 0, 0, 0, 0, 0};
    bf16x8 kf0 = KLD0(lds);
    bf16x4 kf1 = *(const LDSP bf16x4*)(lds + koff1);
    {
      f32x16 s = MFMA32(kf0, q0, zero);
      s = MFMA32K8(kf1, q1, s);
      float tm = s[0];
#pragma unroll
      for (int e = 1; e < 16; ++e) tm = fmaxf(tm, s[e]);
      tm = xhalf_max(tm);
      if (hh) q1[0] = (short)(pk2(-tm, 0.f) & 0xffffu);
    }
    f32x16 sA = MFMA32(kf0, q0, zero), sB;
    sA = MFMA32K8(kf1, q1, sA);
    kf0 = KLD0(lds + KT);
    kf1 = *(const LDSP bf16x4*)(lds + KT + koff1);
    u32x4 p0, p1 = {0u, 0u, 0u, 0u};
    bf16x8 vf0, vf1 = {0, 0, 0, 0, 0, 0, 0, 0};
#define SB() __builtin_amdgcn_sched_barrier(0)
#define PIN(x) asm volatile("" : "+v"(x))
#define EX(S, i) S[i] = __builtin_amdgcn_exp2f(S[i])
#define TILE(SC, SN, VP, KP)                                                                   \
  do {                                                                                         \
    EX(SC, 0); EX(SC, 1); SB();                                                                \
    o = MFMA32(vf1, __builtin_bit_cast(bf16x8, p1), o); SB();                                  \
    vf0 = *(const LDSP bf16x8*)((VP)); vf1 = *(const LDSP bf16x8*)((VP) + VS);                \
    EX(SC, 2); EX(SC, 3); p0[0] = pk2(SC[0], SC[1]); p0[1] = pk2(SC[2], SC[3]);                \
    EX(SC, 4); EX(SC, 5); SB();                                                                \
    SN = MFMA32(kf0, q0, zero); SB();                                                          \
    EX(SC, 6); EX(SC, 7); p0[2] = pk2(SC[4], SC[5]); p0[3] = pk2(SC[6], SC[7]);                \
    EX(SC, 8); EX(SC, 9); SB();                                                                \
    SN = MFMA32K8(kf1, q1, SN); SB();                                                          \
    kf0 = KLD0(KP); kf1 = *(const LDSP bf16x4*)((KP) + koff1);      \
    EX(SC, 10); EX(SC, 11); p1[0] = pk2(SC[8], SC[9]); p1[1] = pk2(SC[10], SC[11]);            \
    EX(SC, 12); EX(SC, 13); SB();                                                              \
    o = MFMA32(vf0, __builtin_bit_cast(bf16x8, p0), o); SB();                                  \
    EX(SC, 14); EX(SC, 15); p1[2] = pk2(SC[12], SC[13]); p1[3] = pk2(SC[14], SC[15]); SB();    \
  } while (0)
    int slot = 0;
    TK(8);
    for (int stage = 0; stage < 8; ++stage) {
      const int nslot = slot == 2 ? 0 : slot + 1;
      const LDSP char* cb = lds + slot * KV_STAGE;
      const LDSP char* nb = lds + nslot * KV_STAGE;
      const LDSP char* vb = cb + voff;
      if (stage == 2) {
#pragma unroll
        for (int k = 0; k < 10; ++k) wpre[k] = bload(rw, lane * 16, k * 20480);
        SB();
      }
      TILE(sA, sB, vb + 0 * VT, cb + 2 * KT);
      TILE(sB, sA, vb + 1 * VT, cb + 3 * KT);
      TILE(sA, sB, vb + 2 * VT, cb + 4 * KT);
      TILE(sB, sA, vb + 3 * VT, cb + 5 * KT);
      TILE(sA, sB, vb + 4 * VT, cb + 6 * KT);
      const unsigned full = lds_ld(lds + K2_FLAGS + ((stage + 1) & 7) * 4);
      SB();
      TILE(sB, sA, vb + 5 * VT, cb + 7 * KT);
      if (stage == 0) TK(2); if (stage == 3) TK(3); if (stage == 6) TK(4); if (stage == 7) TK(5);
      if (stage < 7 && (unsigned)__builtin_amdgcn_readfirstlane(full) < 4u) lds_wait(lds0 + K2_FLAGS + (stage + 1) * 4, 4u);
      asm volatile("" ::: "memory");
      TILE(sA, sB, vb + 6 * VT, nb + 0 * KT);
      TILE(sB, sA, vb + 7 * VT, nb + 1 * KT);
      lds_inc(lds0 + K2_FLAGS + 32 + stage * 4);
      slot = nslot;
    }
    o = MFMA32(vf1, __builtin_bit_cast(bf16x8, p1), o);
    TK(9);
    {
      float l = __shfl(o[8], 32 + r);
      float amax = 0.f;
#pragma unroll
      for (int e = 0; e < 12; ++e) amax = fmaxf(amax, fabsf(o[e]));
      bool bad = !(l <= 1.0e36f) || !(l >= 8.7e-19f) || !(amax <= 3.0e38f);
#pragma unroll
      for (int e = 0; e < 12; ++e) bad = bad || (o[e] != o[e]);
      if (__any(bad)) { if (hh) q1[0] = 0; attn_fallback(ws + QK_OFF + (size_t)(160 + head * 5) * 16384, ws + V_OFF + (size_t)head * 64 * VT, q0, q1, lane, o); }
    }
    const float l = __shfl(o[8], 32 + r);
    const float inv = 1.f / l;
    const rsrc_t rws = mk_rsrc(ws);
    const unsigned odst = (unsigned)OF_OFF + rt * 40 * 1024;
#pragma unroll
    for (int e = 0; e < 12; ++e) {
      const float v = o[e] * inv;
      o[e] = (v != v) ? __uint_as_float(0x7fc00000u) : v;
    }
    u32x4 om;
#pragma unroll
    for (int e = 0; e < 4; ++e) om[e] = pk2(o[2 * e], o[2 * e + 1]);
    __builtin_amdgcn_raw_buffer_store_b128(om, rws, odst + head * 1024 + lane * 16, 0, 16);
    if (hh == 0) {
      u32x2 orr;
      orr[0] = pk2(o[8], o[9]);
      orr[1] = pk2(o[10], o[11]);
      __builtin_amdgcn_raw_buffer_store_b64(orr, rws, odst + (32 + (head >> 2)) * 1024 + (((head >> 1) & 1) * 32 + r) * 16 + (head & 1) * 8, 0, 16);
    }
    TK(10);
    const rsrc_t ro = mk_rsrc(ws + OF_OFF + (size_t)jrt * 40 * 1024);
    LDSP char* pb = lds + K2_RING + kq * PART_SZ + r * PART_ROW + hh * 16 + tg * 2 * 128;
    const float gj = *(const float*)(ws + G_OFF + (size_t)(jrt * 32 + r) * 4);
    if (tg == 0) oproj<1, 1, true>(ro, rw, lane * 16, wpre, lds, lds0, wid, pb, gj TKPASS);
    else oproj<1, 0, true>(ro, rw, lane * 16, wpre, lds, lds0, wid, pb, gj TKPASS);
  } else {
#define GPRIO 0
#define GSLEEP 0
    __builtin_amdgcn_s_setprio(GPRIO);
    const int kq = wid - 8;
    const int rt = xcd * 8 + (idx >> 2), cg = idx & 3;
    const rsrc_t ra = mk_rsrc(ws + HF_OFF + ((size_t)rt * 108 + kq * 27) * 1024);
    const rsrc_t rb = mk_rsrc(ws + W2F_OFF + ((size_t)kq * 27 * 20 + cg * 5) * 1024);
    const unsigned voff = lane * 16;
    const int head = xcd * 4 + (idx >> 3);
    const char* pbase[6];
    unsigned pstep[6], pdst[6];
#pragma unroll
    for (int i = 0; i < 6; ++i) {
      const int pc = min(kq + 4 * i, 22);
      pdst[i] = lds0 + pc * 1024;
      if (pc < 10) { pbase[i] = ws + QK_OFF + (size_t)(160 + head * 5 + (pc >> 1)) * 16384 + (pc & 1) * 1024; pstep[i] = 2048; }
      else if (pc < 12) { pbase[i] = ws + CB_OFF + (pc - 10) * 1024; pstep[i] = 0; }
      else { pbase[i] = ws + V_OFF + (size_t)head * 8 * VSTAGE + (pc - 12) * 1024; pstep[i] = VSTAGE; }
    }
    auto dma = [&](int stage) {
#pragma unroll
      for (int i = 0; i < 6; ++i) glds16s(pbase[i] + (size_t)stage * pstep[i], voff, pdst[i] + (stage % 3) * KV_STAGE);
    };
    dma(0);
    dma(1);
    f32x16 acc[5];
#pragma unroll
    for (int c = 0; c < 5; ++c)
#pragma unroll
      for (int e = 0; e < 16; ++e) acc[c][e] = 0.f;
    constexpr int PD = 3;
    bf16x8 af[PD], bfr[PD][5];
#pragma unroll
    for (int j = 0; j < PD; ++j) {
      af[j] = bload(ra, voff, j * 1024);
#pragma unroll
      for (int c = 0; c < 5; ++c) bfr[j][c] = bload(rb, voff, j * 20480 + c * 1024);
    }
    __builtin_amdgcn_sched_barrier(0);
    WAITV(24);
    BARRIER();
    __builtin_amdgcn_sched_barrier(0);
#define KPS 3
    auto kstep = [&](int k) {
      const int j = k % PD;
#pragma unroll
      for (int c = 0; c < 5; ++c) acc[c] = MFMA32(bfr[j][c], af[j], acc[c]);
      __builtin_amdgcn_sched_barrier(0);
      if (k + PD < 27) {
        af[j] = bload(ra, voff, (k + PD) * 1024);
#pragma unroll
        for (int c = 0; c < 5; ++c) bfr[j][c] = bload(rb, voff, (k + PD) * 20480 + c * 1024);
      }
      __builtin_amdgcn_sched_barrier(0);
    };
#pragma unroll
    for (int stage = 0; stage < 7; ++stage) {
#pragma unroll
      for (int i = 0; i < KPS; ++i) {
        kstep(stage * KPS + i);
        if (i == 1) {
          WAITV(12);
          lds_inc(lds0 + K2_FLAGS + (stage + 1) * 4);
          __builtin_amdgcn_sched_barrier(0);
        }
      }
      TK(2 + stage);
      if (stage + 2 < 8) {
        if (stage >= 1) lds_wait(lds0 + K2_FLAGS + 32 + (stage - 1) * 4, 8u);
        dma(stage + 2);
      }
      __builtin_amdgcn_sched_barrier(0);
    }
    lds_wait(lds0 + K2_FLAGS + 32 + 7 * 4, 4u);
    TK(9);
#pragma unroll
    for (int k = 7 * KPS; k < 27; ++k) kstep(k);
    {
      unsigned vy = voff;
      asm volatile("" : "+v"(vy));
      const int l2 = vy >> 4;
      const float gj = *(const float*)(ws + G_OFF + (size_t)(rt * 32 + (l2 & 31)) * 4);
      const float h = 1.f - gj;
      LDSP char* pb = lds + K2_RING + kq * PART_SZ + (l2 & 31) * PART_ROW + (l2 >> 5) * 16;
#pragma unroll
      for (int c = 0; c < 5; ++c)
#pragma unroll
        for (int g = 0; g < 4; ++g) {
          f32x4 v = {h * acc[c][4 * g], h * acc[c][4 * g + 1], h * acc[c][4 * g + 2], h * acc[c][4 * g + 3]};
          *(LDSP f32x4*)(pb + c * 128 + g * 32) = v;
        }
      TK(10);
      const rsrc_t ro = mk_rsrc(ws + OF_OFF + (size_t)rt * 40 * 1024);
      const rsrc_t rw = mk_rsrc(ws + WOF_OFF + ((size_t)kq * 10 * 20 + cg * 5 + 3) * 1024);
      bf16x8 none[10];
      oproj<0, 2, false>(ro, rw, vy, none, lds, lds0, wid, pb + 3 * 128, gj TKPASS);
    }
  }
  BARRIER();
  TK(14);
  {
    const int jrt = xcd * 8 + (idx >> 2), cg = idx & 3;
    if (tid < 640) {
      const int row = tid / 20, c8 = tid - row * 20;
      const LDSP char* p = lds + K2_RING + row * PART_ROW + c8 * 32;
      f32x4 s0 = *(const LDSP f32x4*)(p), s1 = *(const LDSP f32x4*)(p + 16);
#pragma unroll
      for (int w = 1; w < 4; ++w) {
        s0 += *(const LDSP f32x4*)(p + w * PART_SZ);
        s1 += *(const LDSP f32x4*)(p + w * PART_SZ + 16);
      }
      const u32x4 o = {pk2(s0[0], s0[1]), pk2(s0[2], s0[3]), pk2(s1[0], s1[1]), pk2(s1[2], s1[3])};
      *(u32x4*)(out + (size_t)(jrt * 32 + row) * 640 + cg * 160 + c8 * 8) = o;
    }
  }
  STAMP(1, 15);
}

extern "C" void kernel_launch(void* const* d_in, const int* in_sizes, int n_in, void* d_out, int out_size, void* d_ws,
                              size_t ws_size, hipStream_t stream) {
  const u16* x = (const u16*)d_in[0];
  const u16* wqkv = (const u16*)d_in[1];
  const u16* wo = (const u16*)d_in[2];
  const u16* w1 = (const u16*)d_in[4];
  const u16* w2 = (const u16*)d_in[5];
  const u16* gw = (const u16*)d_in[6];
  char* ws = (char*)d_ws;
  k1_kernel<<<256, 768, 0, stream>>>(x, wqkv, w1, w2, wo, gw, ws);
  k2_kernel<<<256, 768, 0, stream>>>(ws, (u16*)d_out);
}
```

```cpp
#include <hip/hip_runtime.h>
#include <hip/hip_bf16.h>
#include <stdint.h>

typedef __attribute__((ext_vector_type(8))) short bf16x8;
typedef __attribute__((ext_vector_type(4))) short bf16x4;
typedef __attribute__((ext_vector_type(16))) float f32x16;
typedef __attribute__((ext_vector_type(4))) float f32x4;
typedef __attribute__((ext_vector_type(2))) float f32x2;
typedef __attribute__((ext_vector_type(4))) unsigned u32x4;
typedef __attribute__((ext_vector_type(2))) unsigned u32x2;
typedef __attribute__((ext_vector_type(2))) __bf16 bf16x2_t;
typedef unsigned short u16;
#define LDSP __attribute__((address_space(3)))
#define DI __device__ __forceinline__

constexpr int SEQ = 2048, DM = 640, NH = 32, DFF = 1728;
constexpr int KT = 256, VT = 1408, VS = 704, VH = 352;
constexpr int KV_VOFF = 6 * 2048;
constexpr int VSTAGE = 8 * VT;
constexpr int KV_STAGE = KV_VOFF + VSTAGE;
constexpr size_t QK_OFF = 0;
constexpr size_t CB_OFF = QK_OFF + (size_t)320 * 16384;
constexpr size_t V_OFF = CB_OFF + 2048;
constexpr size_t HF_OFF = V_OFF + (size_t)32 * 8 * VSTAGE;
constexpr size_t W2F_OFF = HF_OFF + (size_t)64 * 108 * 1024;
constexpr size_t WOF_OFF = W2F_OFF + (size_t)108 * 20 * 1024;
constexpr size_t OF_OFF = WOF_OFF + (size_t)40 * 20 * 1024;
constexpr size_t G_OFF = OF_OFF + (size_t)64 * 40 * 1024;

#define TK(i)
#define TKARG
#define TKPASS
#define STAMP(kid, slot)
#define DIAGWS
#define MFMA32(a, b, c) __builtin_amdgcn_mfma_f32_32x32x16_bf16((a), (b), (c), 0, 0, 0)
#define MFMA32K8(a, b, c) __builtin_amdgcn_mfma_f32_32x32x8bf16_1k((a), (b), (c), 0, 0, 0)

DI unsigned pk2(float lo, float hi) {
  f32x2 v = {lo, hi};
  bf16x2_t r = __builtin_convertvector(v, bf16x2_t);
  return __builtin_bit_cast(unsigned, r);
}
DI float bf_lo(unsigned u) { return __uint_as_float(u << 16); }
DI float bf_hi(unsigned u) { return __uint_as_float(u & 0xffff0000u); }

DI void glds16(const void* g, unsigned lds_dst) {
  unsigned sv;
  asm volatile("s_mov_b32 %0, m0\n\ts_mov_b32 m0, %2\n\ts_nop 0\n\tglobal_load_lds_dwordx4 %1, off\n\ts_mov_b32 m0, %0"
               : "=&s"(sv) : "v"(g), "s"(lds_dst) : "memory");
}
DI void glds16s(const void* sbase, unsigned voff, unsigned lds_dst) {
  unsigned sv;
  asm volatile("s_mov_b32 %0, m0\n\ts_mov_b32 m0, %3\n\ts_nop 0\n\tglobal_load_lds_dwordx4 %1, %2\n\ts_mov_b32 m0, %0"
               : "=&s"(sv) : "v"(voff), "s"(sbase), "s"(lds_dst) : "memory");
}
typedef __amdgpu_buffer_rsrc_t rsrc_t;
DI rsrc_t mk_rsrc(const void* p) { return __builtin_amdgcn_make_buffer_rsrc((void*)p, 0, 0x7fffffff, 0x00020000); }
DI bf16x8 bload(rsrc_t r, unsigned voff, unsigned soff) {
  return __builtin_bit_cast(bf16x8, __builtin_amdgcn_raw_buffer_load_b128(r, voff, soff, 0));
}
#define STAUX 0
DI void st16(rsrc_t r, unsigned off, u32x4 v) { __builtin_amdgcn_raw_buffer_store_b128(v, r, off, 0, STAUX); }
#define STAUXH 0
DI void st16h(rsrc_t r, unsigned off, u32x4 v) { __builtin_amdgcn_raw_buffer_store_b128(v, r, off, 0, STAUXH); }
DI void st8(rsrc_t r, unsigned off, u32x2 v) { __builtin_amdgcn_raw_buffer_store_b64(v, r, off, 0, STAUX); }
DI bf16x4 tr_read(const LDSP char* p) {
  return __builtin_amdgcn_ds_read_tr16_b64_v4i16((LDSP bf16x4*)p);
}
#define WAITV(n) asm volatile("s_waitcnt vmcnt(" #n ")" ::: "memory")
#define BARRIER() asm volatile("s_waitcnt lgkmcnt(0)\n\ts_barrier" ::: "memory")

constexpr int K1_SLOT = 49152;

template <bool TR>
DI void k1_mainloop(f32x16 (&acc)[2][2], LDSP char* lds, const int (&aoff)[4], const int (&boff)[2]
                    ) {
  bf16x8 fa[2][2], fb[2][2];
  auto rd = [&](int buf, int kt, int ks) {
    const LDSP char* sa = lds + (kt % 3) * K1_SLOT;
    const LDSP char* sb = sa + 32768;
#pragma unroll
    for (int nt = 0; nt < 2; ++nt) {
      bf16x4 lo = tr_read(sb + boff[nt] + ks * 4096);
      bf16x4 hi = tr_read(sb + boff[nt] + ks * 4096 + 1024);
      fb[buf][nt] = __builtin_shufflevector(lo, hi, 0, 1, 2, 3, 4, 5, 6, 7);
    }
#pragma unroll
    for (int mt = 0; mt < 2; ++mt) fa[buf][mt] = *(const LDSP bf16x8*)(sa + aoff[ks] + mt * 4096);
  };
  BARRIER();
  rd(0, 0, 0);
  __builtin_amdgcn_sched_barrier(0);
#pragma unroll
  for (int kt = 0; kt < 10; ++kt) {
#pragma unroll
    for (int ks = 0; ks < 4; ++ks) {
      const int cb = ks & 1;
      if (ks < 3) rd(cb ^ 1, kt, ks + 1);
      else if (kt < 9) {
        BARRIER();
        rd(cb ^ 1, kt + 1, 0);
      }
      __builtin_amdgcn_sched_barrier(0);
#pragma unroll
      for (int mt = 0; mt < 2; ++mt)
#pragma unroll
        for (int nt = 0; nt < 2; ++nt)
          acc[mt][nt] = TR ? MFMA32(fb[cb][nt], fa[cb][mt], acc[mt][nt]) : MFMA32(fa[cb][mt], fb[cb][nt], acc[mt][nt]);
      __builtin_amdgcn_sched_barrier(0);
    }
  }
}

DI void k1_loader(int lw, int lane, unsigned lds0, const u16* xa, const u16* wb, int ldw, bool half_tile) {
  unsigned aoffs[2], boffs;
#pragma unroll
  for (int par = 0; par < 2; ++par) aoffs[par] = (lane >> 3) * 1280 + (((lane & 7) ^ ((4 * par + (lane >> 4)) & 7)) * 16);
  {
    int c = (lane & 15) ^ (((lane >> 4) & 3) << 2);
    if (half_tile) c &= 7;
    boffs = (lane >> 4) * ldw * 2 + c * 16;
  }
  auto issue = [&](int kt) {
    const unsigned sl = lds0 + (kt % 3) * K1_SLOT;
#pragma unroll
    for (int i = 0; i < 8; ++i) {
      const int pc = lw * 8 + i;
      glds16s(xa + (size_t)pc * 8 * 640 + kt * 64, aoffs[i & 1], sl + pc * 1024);
    }
#pragma unroll
    for (int i = 0; i < 4; ++i) {
      const int pc = lw * 4 + i;
      glds16s(wb + (size_t)(kt * 64 + pc * 4) * ldw, boffs, sl + 32768 + pc * 1024);
    }
  };
  issue(0);
  issue(1);
  WAITV(12);
  BARRIER();
  issue(2);
#pragma unroll
  for (int kt = 0; kt < 9; ++kt) {
    if (kt < 8) WAITV(12); else WAITV(0);
    BARRIER();
    if (kt + 3 < 10) issue(kt + 3);
  }
}

DI void k1_aux(int aux, LDSP char* lds, const u16* x, const u16* w2, const u16* wo, const u16* gw, char* ws) {
  const int tid = threadIdx.x, lane = tid & 63, wid = __builtin_amdgcn_readfirstlane(tid >> 6);
  const unsigned lds0 = (unsigned)(uintptr_t)lds;
  const rsrc_t rws = mk_rsrc(ws);
  {
    const u32x4 ones = {0x3f803f80u, 0x3f803f80u, 0x3f803f80u, 0x3f803f80u}, zeros = {0u, 0u, 0u, 0u};
    if (aux == 0 && tid < 128) st16(rws, (unsigned)CB_OFF + tid * 16, u32x4{0x00003f80u, 0u, 0x00003f80u, 0u});
    for (int i = aux * 768 + tid; i < 2048 * 8; i += 24 * 768)
      st16(rws, (unsigned)V_OFF + (i >> 3) * VT + ((i & 7) >> 1) * VH + (20 + (i & 1)) * 16, (i & 1) ? zeros : ones);
  }
  {
    const u32x4 ff = {0xffffffffu, 0xffffffffu, 0xffffffffu, 0xffffffffu};
    for (int i = aux * 768 + tid; i < 64 * 40 * 64; i += 24 * 768) st16(rws, (unsigned)OF_OFF + i * 16, ff);
  }
  constexpr int SLAB = 16 * 1344;
  for (int pc = wid; pc < 147; pc += 12) {
    const int sl = pc / 21, pp = pc - sl * 21;
    int ksg = aux + sl * 24;
    if (ksg >= 148) ksg = 147;
    const int i = pp * 64 + lane, rho = i / 84;
    int c = i - rho * 84;
    if (c > 79) c = 79;
    const bool isw2 = ksg < 108;
    const int ks = isw2 ? ksg : ksg - 108;
    int row;
    if (isw2) row = 16 * ks + rho;
    else if (ks < 32) row = 20 * ks + rho;
    else row = 20 * (4 * (ks - 32) + 2 * ((rho >> 2) & 1) + (rho >> 3)) + 16 + (rho & 3);
    glds16((isw2 ? w2 : wo) + (size_t)row * 640 + c * 8, lds0 + pc * 1024);
  }
  const int wv = aux * 12 + wid, sub = lane >> 4, l16 = lane & 15;
  u32x4 g4[5], x4[2][5];
#pragma unroll
  for (int c = 0; c < 5; ++c) g4[c] = *(const u32x4*)(gw + (c * 16 + l16) * 8);
#pragma unroll
  for (int i = 0; i < 2; ++i) {
    int row = wv * 4 + i * 1152 + sub;
    if (row >= SEQ) row = SEQ - 1;
#pragma unroll
    for (int c = 0; c < 5; ++c) x4[i][c] = *(const u32x4*)(x + (size_t)row * 640 + (c * 16 + l16) * 8);
  }
  float* gout = (float*)(ws + G_OFF);
#pragma unroll
  for (int i = 0; i < 2; ++i) {
    const int row = wv * 4 + i * 1152 + sub;
    float s = 0.f;
#pragma unroll
    for (int c = 0; c < 5; ++c)
#pragma unroll
      for (int e = 0; e < 4; ++e) { s += bf_lo(x4[i][c][e]) * bf_lo(g4[c][e]); s += bf_hi(x4[i][c][e]) * bf_hi(g4[c][e]); }
    s += __shfl_xor(s, 1); s += __shfl_xor(s, 2); s += __shfl_xor(s, 4); s += __shfl_xor(s, 8);
    if (l16 == 0 && row < SEQ) gout[row] = 1.f / (1.f + __expf(-s));
  }
  WAITV(0);
  BARRIER();
  const int toff = (4 * (lane >> 5) + ((lane & 15) >> 2)) * 1344 + ((lane >> 4) & 1) * 32 + (lane & 3) * 8;
  for (int f = wid; f < 140; f += 12) {
    const int sl = f / 20, ct = f - sl * 20;
    const int ksg = aux + sl * 24;
    if (ksg >= 148) break;
    const LDSP char* pb = lds + sl * SLAB + toff + ct * 64;
    bf16x4 lo = tr_read(pb), hi = tr_read(pb + 8 * 1344);
    bf16x8 v = __builtin_shufflevector(lo, hi, 0, 1, 2, 3, 4, 5, 6, 7);
    st16(rws, (unsigned)W2F_OFF + (ksg * 20 + ct) * 1024 + lane * 16, __builtin_bit_cast(u32x4, v));
  }
}

__global__ __launch_bounds__(768) void k1_kernel(const u16* __restrict__ x, const u16* __restrict__ wqkv,
                                                 const u16* __restrict__ w1, const u16* __restrict__ w2,
                                                 const u16* __restrict__ wo, const u16* __restrict__ gw,
                                                 char* __restrict__ ws) {
  __shared__ __attribute__((aligned(16))) char lds_raw[147 * 1024];
  const int bid = blockIdx.x, xcd = bid & 7, idx = bid >> 3;
  STAMP(0, 0);
  if (idx >= 29) { k1_aux(xcd * 3 + (idx - 29), (LDSP char*)lds_raw, x, w2, wo, gw, ws); STAMP(0, 2); return; }
  const int ii = (xcd & 3) * 29 + idx, nti = ii >> 2, mti = (xcd >> 2) * 4 + (ii & 3);
  const int tid = threadIdx.x, lane = tid & 63, wid = __builtin_amdgcn_readfirstlane(tid >> 6);
  const int r = lane & 31, hh = lane >> 5, wm = wid >> 1, wn = wid & 1;
  const int m0 = mti * 256;
  const bool isq = nti < 15;
  const u16* W = isq ? wqkv : w1;
  const int ldw = isq ? 1920 : DFF;
  const int n0 = isq ? nti * 128 : (nti - 15) * 128;
  const bool half_tile = (nti == 28);
  LDSP char* lds = (LDSP char*)lds_raw;
  const unsigned lds0 = (unsigned)(uintptr_t)lds;

  if (wid >= 8) { k1_loader(wid - 8, lane, lds0, x + (size_t)m0 * 640, W + n0, ldw, half_tile); return; }
  int aoff[4], boff[2];
#pragma unroll
  for (int ks = 0; ks < 4; ++ks) aoff[ks] = (wm * 64 + r) * 128 + (((2 * ks + hh) ^ ((r >> 1) & 7)) * 16);
  {
    const int q = (lane & 15) >> 2, p = lane & 3, blk = (lane >> 4) & 1;
#pragma unroll
    for (int nt = 0; nt < 2; ++nt)
      boff[nt] = (8 * hh + q) * 256 + ((((wn * 2 + nt) ^ q) * 4 + 2 * blk + (p >> 1)) * 16) + 8 * (p & 1);
  }
  f32x16 acc[2][2];
#pragma unroll
  for (int mt = 0; mt < 2; ++mt)
#pragma unroll
    for (int nt = 0; nt < 2; ++nt)
#pragma unroll
      for (int e = 0; e < 16; ++e) acc[mt][nt][e] = 0.f;

  const bool isv = (nti >= 10 && nti < 15);
  const rsrc_t rws = mk_rsrc(ws);
  const int rb0 = m0 + wm * 64;
  unsigned dsto[2] = {0u, 0u};
  float sc = 1.f;
  if (isv) {
#pragma unroll
    for (int nt = 0; nt < 2; ++nt) {
      const int col = (nti - 10) * 128 + wn * 64 + nt * 32 + r, head = col / 20, d = col - head * 20;
      dsto[nt] = (unsigned)V_OFF + (head * 64 + (rb0 >> 5)) * VT + hh * VH + d * 16;
    }
  } else if (nti < 10) {
    if (nti < 5) sc = 0.22360679774997896f * 1.4426950408889634f;
    dsto[0] = (unsigned)QK_OFF + (nti * 32 + wn * 16 + hh) * 16384 + (rb0 + r) * 8;
  } else {
    dsto[0] = (unsigned)HF_OFF + ((rb0 >> 5) * 108 + (((nti - 15) * 128 + wn * 64) >> 4)) * 1024 + lane * 16;
    if (half_tile && wn == 1) dsto[0] = 0x80000000u;
  }
  const unsigned da = dsto[0], db = dsto[1];
  if (isv) {
    k1_mainloop<false>(acc, lds, aoff, boff DIAGWS);
    STAMP(0, 1);
    {
#pragma unroll
      for (int nt = 0; nt < 2; ++nt)
#pragma unroll
        for (int mt = 0; mt < 2; ++mt)
#pragma unroll
          for (int s = 0; s < 2; ++s) {
            u32x4 o;
#pragma unroll
            for (int e = 0; e < 4; ++e) o[e] = pk2(acc[mt][nt][8 * s + 2 * e], acc[mt][nt][8 * s + 2 * e + 1]);
            __builtin_amdgcn_raw_buffer_store_b128(o, rws, nt ? db : da, mt * VT + s * VS, 0);
          }
    }
  } else {
    k1_mainloop<true>(acc, lds, aoff, boff DIAGWS);
    STAMP(0, 1);
    {
      if (nti < 10) {
#pragma unroll
        for (int nt = 0; nt < 2; ++nt)
#pragma unroll
          for (int g = 0; g < 4; ++g)
#pragma unroll
            for (int mt = 0; mt < 2; ++mt) {
              u32x2 o;
              o[0] = pk2(acc[mt][nt][4 * g] * sc, acc[mt][nt][4 * g + 1] * sc);
              o[1] = pk2(acc[mt][nt][4 * g + 2] * sc, acc[mt][nt][4 * g + 3] * sc);
              __builtin_amdgcn_raw_buffer_store_b64(o, rws, da, (nt * 8 + g * 2) * 16384 + mt * 256, 0);
            }
      } else {
#pragma unroll
        for (int nt = 0; nt < 2; ++nt)
#pragma unroll
          for (int mt = 0; mt < 2; ++mt)
#pragma unroll
            for (int s = 0; s < 2; ++s) {
              u32x4 o;
#pragma unroll
              for (int e = 0; e < 4; ++e)
                o[e] = pk2(fmaxf(acc[mt][nt][8 * s + 2 * e], 0.f), fmaxf(acc[mt][nt][8 * s + 2 * e + 1], 0.f));
              __builtin_amdgcn_raw_buffer_store_b128(o, rws, da, mt * (108 * 1024) + (nt * 2 + s) * 1024, 0);
            }
      }
    }
  }
  STAMP(0, 2);
}

constexpr int K2_RING = 3 * KV_STAGE;
constexpr int PART_ROW = 656;
constexpr int PART_SZ = 32 * PART_ROW;
constexpr int K2_FLAGS = K2_RING + 4 * PART_SZ;
constexpr int K2_LDS = K2_FLAGS + 128;

DI void pack_p(const f32x16& s, bf16x8& p0, bf16x8& p1) {
  u32x4 a, b;
#pragma unroll
  for (int e = 0; e < 4; ++e) { a[e] = pk2(s[2 * e], s[2 * e + 1]); b[e] = pk2(s[8 + 2 * e], s[8 + 2 * e + 1]); }
  p0 = __builtin_bit_cast(bf16x8, a); p1 = __builtin_bit_cast(bf16x8, b);
}
DI float xhalf_max(float m) { return fmaxf(m, __shfl_xor(m, 32)); }

DI unsigned lds_ld(LDSP char* p) { return __hip_atomic_load((LDSP unsigned*)p, __ATOMIC_RELAXED, __HIP_MEMORY_SCOPE_WORKGROUP); }
DI void lds_inc(unsigned addr) {
  unsigned va, v1; unsigned long long sv;
  asm volatile("v_mov_b32 %0, %3\n\tv_mov_b32 %1, 1\n\ts_mov_b64 %2, exec\n\ts_mov_b64 exec, 1\n\t"
               "ds_add_u32 %0, %1\n\ts_mov_b64 exec, %2"
               : "=&v"(va), "=&v"(v1), "=&s"(sv) : "s"(addr) : "memory");
}
DI void lds_wait(unsigned addr, unsigned need) {
  unsigned va, vv, sv;
  asm volatile("v_mov_b32 %0, %3\n"
               "LW%=:\n\tds_read_b32 %1, %0\n\ts_waitcnt lgkmcnt(0)\n\tv_readfirstlane_b32 %2, %1\n\ts_nop 3\n\t"
               "s_cmp_ge_u32 %2, %4\n\ts_cbranch_scc1 LD%=\n\ts_sleep 1\n\ts_branch LW%=\n"
               "LD%=:"
               : "=&v"(va), "=&v"(vv), "=&s"(sv) : "s"(addr), "s"(need) : "memory", "scc");
}

DI void attn_fallback(const char* kh, const char* vh, bf16x8 q0, bf16x4 q1, int lane, f32x16& o_out) {
  const int r = lane & 31, hh = lane >> 5, rv = r < 21 ? r : 21;
  f32x16 o, zero;
#pragma unroll
  for (int e = 0; e < 16; ++e) { o[e] = 0.f; zero[e] = 0.f; }
  float m = -INFINITY;
  for (int t = 0; t < 64; ++t) {
    const char* kp = kh + (t * 32 + r) * 8;
    const bf16x4 ka = *(const bf16x4*)(kp + (2 * hh) * 16384), kb = *(const bf16x4*)(kp + (2 * hh + 1) * 16384);
    const bf16x8 kf0 = __builtin_shufflevector(ka, kb, 0, 1, 2, 3, 4, 5, 6, 7);
    const bf16x4 kf1 = *(const bf16x4*)(kp + 4 * 16384);
    const bf16x8 vf0 = *(const bf16x8*)(vh + t * VT + hh * VH + rv * 16);
    const bf16x8 vf1 = *(const bf16x8*)(vh + t * VT + VS + hh * VH + rv * 16);
    f32x16 s = MFMA32(kf0, q0, zero);
    s = MFMA32K8(kf1, q1, s);
    float tm = s[0];
#pragma unroll
    for (int e = 1; e < 16; ++e) tm = fmaxf(tm, s[e]);
    tm = xhalf_max(tm);
    const float mn = fmaxf(m, tm);
    const float alpha = __builtin_amdgcn_exp2f(m - mn);
    m = mn;
#pragma unroll
    for (int e = 0; e < 16; ++e) { o[e] *= alpha; s[e] = __builtin_amdgcn_exp2f(s[e] - mn); }
    bf16x8 p0, p1;
    pack_p(s, p0, p1);
    o = MFMA32(vf0, p0, o);
    o = MFMA32(vf1, p1, o);
  }
  o_out = o;
}

DI bool frag_ready(const bf16x8& f) {
  const u32x4 w = __builtin_bit_cast(u32x4, f);
  unsigned m = 0xffffu;
#pragma unroll
  for (int e = 0; e < 4; ++e) { const unsigned t = ~w[e]; m = min(m, min(t & 0xffffu, t >> 16)); }
  return __all(m != 0u);
}
template <int NPRE, int NL, bool POLL>
DI void oproj(rsrc_t ro, rsrc_t rw, unsigned voff, const bf16x8 (&pre)[10], LDSP char* lds, unsigned lds0, int wid,
              LDSP char* pb, float gj TKARG) {
  constexpr int NT = NPRE + NL;
  bf16x8 bfr[10][NL ? NL : 1];
#pragma unroll
  for (int k = 0; k < 10; ++k)
#pragma unroll
    for (int c = 0; c < NL; ++c) bfr[k][c] = bload(rw, voff, k * 20480 + (NPRE + c) * 1024);
  __builtin_amdgcn_sched_barrier(0);
  TK(11);
  if (POLL) {
    lds_wait(lds0 + K2_FLAGS + 32 + 7 * 4, 8u);
    TK(12);
    unsigned pend = 0x1fu;
    bf16x8 of[5];
    for (int spin = 0; pend != 0u && spin < (1 << 18); ++spin) {
#pragma unroll
      for (int i = 0; i < 5; ++i)
        if ((pend >> i) & 1u) of[i] = __builtin_bit_cast(bf16x8, __builtin_amdgcn_raw_buffer_load_b128(ro, voff, (wid + 8 * i) * 1024, 16));
#pragma unroll
      for (int i = 0; i < 5; ++i)
        if (((pend >> i) & 1u) && frag_ready(of[i])) {
          pend &= ~(1u << i);
          *(LDSP bf16x8*)(lds + (wid + 8 * i) * 1024 + voff) = of[i];
        }
    }
  }
  lds_inc(lds0 + K2_FLAGS + 72);
  lds_wait(lds0 + K2_FLAGS + 72, 12u);
  TK(13);
  const LDSP char* ab = lds + (wid & 3) * 10240 + voff;
  f32x16 acc[NT];
#pragma unroll
  for (int c = 0; c < NT; ++c)
#pragma unroll
    for (int e = 0; e < 16; ++e) acc[c][e] = 0.f;
#pragma unroll
  for (int k = 0; k < 10; ++k) {
    const bf16x8 af = *(const LDSP bf16x8*)(ab + k * 1024);
    if (NPRE) acc[0] = MFMA32(pre[k], af, acc[0]);
#pragma unroll
    for (int c = 0; c < NL; ++c) acc[NPRE + c] = MFMA32(bfr[k][c], af, acc[NPRE + c]);
  }
#pragma unroll
  for (int c = 0; c < NT; ++c)
#pragma unroll
    for (int g = 0; g < 4; ++g) {
      f32x4 v = *(const LDSP f32x4*)(pb + c * 128 + g * 32);
#pragma unroll
      for (int e = 0; e < 4; ++e) v[e] += gj * acc[c][4 * g + e];
      *(LDSP f32x4*)(pb + c * 128 + g * 32) = v;
    }
}

__global__ __launch_bounds__(768) void k2_kernel(char* __restrict__ ws, u16* __restrict__ out) {
  __shared__ __attribute__((aligned(16))) char lds_raw[K2_LDS];
  LDSP char* lds = (LDSP char*)lds_raw;
  const unsigned lds0 = (unsigned)(uintptr_t)lds;
  const int bid = blockIdx.x, xcd = bid & 7, idx = bid >> 3;
  const int tid = threadIdx.x, lane = tid & 63, wid = __builtin_amdgcn_readfirstlane(tid >> 6);
  const int r = lane & 31, hh = lane >> 5;
  STAMP(1, 0);
  if (wid < 8) {
    const int head = xcd * 4 + (idx >> 3), qt = idx & 7;
    const int rt = qt * 8 + wid;
    const char* qsrc = ws + QK_OFF + (size_t)head * 5 * 16384 + (rt * 32 + r) * 8;
    const bf16x4 qa = *(const bf16x4*)(qsrc + (2 * hh) * 16384), qb = *(const bf16x4*)(qsrc + (2 * hh + 1) * 16384);
    const bf16x8 q0 = __builtin_shufflevector(qa, qb, 0, 1, 2, 3, 4, 5, 6, 7);
    bf16x4 q1 = *(const bf16x4*)(qsrc + 4 * 16384);
    const int jrt = xcd * 8 + (idx >> 2), cg = idx & 3, kq = wid & 3, tg = wid >> 2;
    const rsrc_t rw = mk_rsrc(ws + WOF_OFF + ((size_t)kq * 10 * 20 + cg * 5 + tg * 2) * 1024);
    if (hh) q1 = bf16x4{0, 0, 0, 0};
    const int rv = r < 21 ? r : 21;
    const int koff0 = hh * 4096 + r * 8, koff1 = (4 + hh) * 2048 + r * 8, voff = KV_VOFF + hh * VH + rv * 16;
#define KLD0(P) __builtin_shufflevector(*(const LDSP bf16x4*)((P) + koff0), *(const LDSP bf16x4*)((P) + koff0 + 2048), 0, 1, 2, 3, 4, 5, 6, 7)
    f32x16 o, zero;
#pragma unroll
    for (int e = 0; e < 16; ++e) { o[e] = 0.f; zero[e] = 0.f; }
    if (tid < 32) *(LDSP unsigned*)(lds + K2_FLAGS + tid * 4) = 0u;
    BARRIER();
    TK(1);
    bf16x8 wpre[10];
#pragma unroll
    for (int k = 0; k < 10; ++k) wpre[k] = bf16x8{0, 0, 0, 0, 0, 0, 0, 0};
    bf16x8 kf0 = KLD0(lds);
    bf16x4 kf1 = *(const LDSP bf16x4*)(lds + koff1);
    {
      f32x16 s = MFMA32(kf0, q0, zero);
      s = MFMA32K8(kf1, q1, s);
      float tm = s[0];
#pragma unroll
      for (int e = 1; e < 16; ++e) tm = fmaxf(tm, s[e]);
      tm = xhalf_max(tm);
      if (hh) q1[0] = (short)(pk2(-tm, 0.f) & 0xffffu);
    }
    f32x16 sA = MFMA32(kf0, q0, zero), sB;
    sA = MFMA32K8(kf1, q1, sA);
    kf0 = KLD0(lds + KT);
    kf1 = *(const LDSP bf16x4*)(lds + KT + koff1);
    u32x4 p0, p1 = {0u, 0u, 0u, 0u};
    bf16x8 vf0, vf1 = {0, 0, 0, 0, 0, 0, 0, 0};
#define SB() __builtin_amdgcn_sched_barrier(0)
#define PIN(x) asm volatile("" : "+v"(x))
#define EX(S, i) S[i] = __builtin_amdgcn_exp2f(S[i])
#define TILE(SC, SN, VP, KP)                                                                   \
  do {                                                                                         \
    EX(SC, 0); EX(SC, 1); SB();                                                                \
    o = MFMA32(vf1, __builtin_bit_cast(bf16x8, p1), o); SB();                                  \
    vf0 = *(const LDSP bf16x8*)((VP)); vf1 = *(const LDSP bf16x8*)((VP) + VS);                \
    EX(SC, 2); EX(SC, 3); p0[0] = pk2(SC[0], SC[1]); p0[1] = pk2(SC[2], SC[3]);                \
    EX(SC, 4); EX(SC, 5); SB();                                                                \
    SN = MFMA32(kf0, q0, zero); SB();                                                          \
    EX(SC, 6); EX(SC, 7); p0[2] = pk2(SC[4], SC[5]); p0[3] = pk2(SC[6], SC[7]);                \
    EX(SC, 8); EX(SC, 9); SB();                                                                \
    SN = MFMA32K8(kf1, q1, SN); SB();                                                          \
    kf0 = KLD0(KP); kf1 = *(const LDSP bf16x4*)((KP) + koff1);      \
    EX(SC, 10); EX(SC, 11); p1[0] = pk2(SC[8], SC[9]); p1[1] = pk2(SC[10], SC[11]);            \
    EX(SC, 12); EX(SC, 13); SB();                                                              \
    o = MFMA32(vf0, __builtin_bit_cast(bf16x8, p0), o); SB();                                  \
    EX(SC, 14); EX(SC, 15); p1[2] = pk2(SC[12], SC[13]); p1[3] = pk2(SC[14], SC[15]); SB();    \
  } while (0)
    int slot = 0;
    TK(8);
    for (int stage = 0; stage < 8; ++stage) {
      const int nslot = slot == 2 ? 0 : slot + 1;
      const LDSP char* cb = lds + slot * KV_STAGE;
      const LDSP char* nb = lds + nslot * KV_STAGE;
      const LDSP char* vb = cb + voff;
      if (stage == 2) {
#pragma unroll
        for (int k = 0; k < 10; ++k) wpre[k] = bload(rw, lane * 16, k * 20480);
        SB();
      }
      TILE(sA, sB, vb + 0 * VT, cb + 2 * KT);
      TILE(sB, sA, vb + 1 * VT, cb + 3 * KT);
      TILE(sA, sB, vb + 2 * VT, cb + 4 * KT);
      TILE(sB, sA, vb + 3 * VT, cb + 5 * KT);
      TILE(sA, sB, vb + 4 * VT, cb + 6 * KT);
      const unsigned full = lds_ld(lds + K2_FLAGS + ((stage + 1) & 7) * 4);
      SB();
      TILE(sB, sA, vb + 5 * VT, cb + 7 * KT);
      if (stage == 0) TK(2); if (stage == 3) TK(3); if (stage == 6) TK(4); if (stage == 7) TK(5);
      if (stage < 7 && (unsigned)__builtin_amdgcn_readfirstlane(full) < 4u) lds_wait(lds0 + K2_FLAGS + (stage + 1) * 4, 4u);
      asm volatile("" ::: "memory");
      TILE(sA, sB, vb + 6 * VT, nb + 0 * KT);
      TILE(sB, sA, vb + 7 * VT, nb + 1 * KT);
      lds_inc(lds0 + K2_FLAGS + 32 + stage * 4);
      slot = nslot;
    }
    o = MFMA32(vf1, __builtin_bit_cast(bf16x8, p1), o);
    TK(9);
    {
      float l = __shfl(o[8], 32 + r);
      float amax = 0.f;
#pragma unroll
      for (int e = 0; e < 12; ++e) amax = fmaxf(amax, fabsf(o[e]));
      bool bad = !(l <= 1.0e36f) || !(l >= 8.7e-19f) || !(amax <= 3.0e38f);
#pragma unroll
      for (int e = 0; e < 12; ++e) bad = bad || (o[e] != o[e]);
      if (__any(bad)) { if (hh) q1[0] = 0; attn_fallback(ws + QK_OFF + (size_t)(160 + head * 5) * 16384, ws + V_OFF + (size_t)head * 64 * VT, q0, q1, lane, o); }
    }
    const float l = __shfl(o[8], 32 + r);
    const float inv = 1.f / l;
    const rsrc_t rws = mk_rsrc(ws);
    const unsigned odst = (unsigned)OF_OFF + rt * 40 * 1024;
#pragma unroll
    for (int e = 0; e < 12; ++e) {
      const float v = o[e] * inv;
      o[e] = (v != v) ? __uint_as_float(0x7fc00000u) : v;
    }
    u32x4 om;
#pragma unroll
    for (int e = 0; e < 4; ++e) om[e] = pk2(o[2 * e], o[2 * e + 1]);
    __builtin_amdgcn_raw_buffer_store_b128(om, rws, odst + head * 1024 + lane * 16, 0, 16);
    if (hh == 0) {
      u32x2 orr;
      orr[0] = pk2(o[8], o[9]);
      orr[1] = pk2(o[10], o[11]);
      __builtin_amdgcn_raw_buffer_store_b64(orr, rws, odst + (32 + (head >> 2)) * 1024 + (((head >> 1) & 1) * 32 + r) * 16 + (head & 1) * 8, 0, 16);
    }
    TK(10);
    const rsrc_t ro = mk_rsrc(ws + OF_OFF + (size_t)jrt * 40 * 1024);
    LDSP char* pb = lds + K2_RING + kq * PART_SZ + r * PART_ROW + hh * 16 + tg * 2 * 128;
    const float gj = *(const float*)(ws + G_OFF + (size_t)(jrt * 32 + r) * 4);
    if (tg == 0) oproj<1, 1, true>(ro, rw, lane * 16, wpre, lds, lds0, wid, pb, gj TKPASS);
    else oproj<1, 0, true>(ro, rw, lane * 16, wpre, lds, lds0, wid, pb, gj TKPASS);
  } else {
#define GPRIO 0
#define GSLEEP 0
    __builtin_amdgcn_s_setprio(GPRIO);
    const int kq = wid - 8;
    const int rt = xcd * 8 + (idx >> 2), cg = idx & 3;
    const rsrc_t ra = mk_rsrc(ws + HF_OFF + ((size_t)rt * 108 + kq * 27) * 1024);
    const rsrc_t rb = mk_rsrc(ws + W2F_OFF + ((size_t)kq * 27 * 20 + cg * 5) * 1024);
    const unsigned voff = lane * 16;
    const int head = xcd * 4 + (idx >> 3);
    const char* pbase[6];
    unsigned pstep[6], pdst[6];
#pragma unroll
    for (int i = 0; i < 6; ++i) {
      const int pc = min(kq + 4 * i, 22);
      pdst[i] = lds0 + pc * 1024;
      if (pc < 10) { pbase[i] = ws + QK_OFF + (size_t)(160 + head * 5 + (pc >> 1)) * 16384 + (pc & 1) * 1024; pstep[i] = 2048; }
      else if (pc < 12) { pbase[i] = ws + CB_OFF + (pc - 10) * 1024; pstep[i] = 0; }
      else { pbase[i] = ws + V_OFF + (size_t)head * 8 * VSTAGE + (pc - 12) * 1024; pstep[i] = VSTAGE; }
    }
    auto dma = [&](int stage) {
#pragma unroll
      for (int i = 0; i < 6; ++i) glds16s(pbase[i] + (size_t)stage * pstep[i], voff, pdst[i] + (stage % 3) * KV_STAGE);
    };
    dma(0);
    dma(1);
    f32x16 acc[5];
#pragma unroll
    for (int c = 0; c < 5; ++c)
#pragma unroll
      for (int e = 0; e < 16; ++e) acc[c][e] = 0.f;
    constexpr int PD = 3;
    bf16x8 af[PD], bfr[PD][5];
#pragma unroll
    for (int j = 0; j < PD; ++j) {
      af[j] = bload(ra, voff, j * 1024);
#pragma unroll
      for (int c = 0; c < 5; ++c) bfr[j][c] = bload(rb, voff, j * 20480 + c * 1024);
    }
    __builtin_amdgcn_sched_barrier(0);
    WAITV(24);
    BARRIER();
    __builtin_amdgcn_sched_barrier(0);
#define KPS 3
    auto kstep = [&](int k) {
      const int j = k % PD;
#pragma unroll
      for (int c = 0; c < 5; ++c) acc[c] = MFMA32(bfr[j][c], af[j], acc[c]);
      __builtin_amdgcn_sched_barrier(0);
      if (k + PD < 27) {
        af[j] = bload(ra, voff, (k + PD) * 1024);
#pragma unroll
        for (int c = 0; c < 5; ++c) bfr[j][c] = bload(rb, voff, (k + PD) * 20480 + c * 1024);
      }
      __builtin_amdgcn_sched_barrier(0);
    };
#pragma unroll
    for (int stage = 0; stage < 7; ++stage) {
#pragma unroll
      for (int i = 0; i < KPS; ++i) {
        kstep(stage * KPS + i);
        if (i == 1) {
          WAITV(12);
          lds_inc(lds0 + K2_FLAGS + (stage + 1) * 4);
          __builtin_amdgcn_sched_barrier(0);
        }
      }
      TK(2 + stage);
      if (stage + 2 < 8) {
        if (stage >= 1) lds_wait(lds0 + K2_FLAGS + 32 + (stage - 1) * 4, 8u);
        dma(stage + 2);
      }
      __builtin_amdgcn_sched_barrier(0);
    }
    lds_wait(lds0 + K2_FLAGS + 32 + 7 * 4, 4u);
    TK(9);
#pragma unroll
    for (int k = 7 * KPS; k < 27; ++k) kstep(k);
    {
      unsigned vy = voff;
      asm volatile("" : "+v"(vy));
      const int l2 = vy >> 4;
      const float gj = *(const float*)(ws + G_OFF + (size_t)(rt * 32 + (l2 & 31)) * 4);
      const float h = 1.f - gj;
      LDSP char* pb = lds + K2_RING + kq * PART_SZ + (l2 & 31) * PART_ROW + (l2 >> 5) * 16;
#pragma unroll
      for (int c = 0; c < 5; ++c)
#pragma unroll
        for (int g = 0; g < 4; ++g) {
          f32x4 v = {h * acc[c][4 * g], h * acc[c][4 * g + 1], h * acc[c][4 * g + 2], h * acc[c][4 * g + 3]};
          *(LDSP f32x4*)(pb + c * 128 + g * 32) = v;
        }
      TK(10);
      const rsrc_t ro = mk_rsrc(ws + OF_OFF + (size_t)rt * 40 * 1024);
      const rsrc_t rw = mk_rsrc(ws + WOF_OFF + ((size_t)kq * 10 * 20 + cg * 5 + 3) * 1024);
      bf16x8 none[10];
      oproj<0, 2, false>(ro, rw, vy, none, lds, lds0, wid, pb + 3 * 128, gj TKPASS);
    }
  }
  BARRIER();
  TK(14);
  {
    const int jrt = xcd * 8 + (idx >> 2), cg = idx & 3;
    if (tid < 640) {
      const int row = tid / 20, c8 = tid - row * 20;
      const LDSP char* p = lds + K2_RING + row * PART_ROW + c8 * 32;
      f32x4 s0 = *(const LDSP f32x4*)(p), s1 = *(const LDSP f32x4*)(p + 16);
#pragma unroll
      for (int w = 1; w < 4; ++w) {
        s0 += *(const LDSP f32x4*)(p + w * PART_SZ);
        s1 += *(const LDSP f32x4*)(p + w * PART_SZ + 16);
      }
      const u32x4 o = {pk2(s0[0], s0[1]), pk2(s0[2], s0[3]), pk2(s1[0], s1[1]), pk2(s1[2], s1[3])};
      *(u32x4*)(out + (size_t)(jrt * 32 + row) * 640 + cg * 160 + c8 * 8) = o;
    }
  }
  STAMP(1, 15);
}

extern "C" void kernel_launch(void* const* d_in, const int* in_sizes, int n_in, void* d_out, int out_size, void* d_ws,
                              size_t ws_size, hipStream_t stream) {
  const u16* x = (const u16*)d_in[0];
  const u16* wqkv = (const u16*)d_in[1];
  const u16* wo = (const u16*)d_in[2];
  const u16* w1 = (const u16*)d_in[4];
  const u16* w2 = (const u16*)d_in[5];
  const u16* gw = (const u16*)d_in[6];
  char* ws = (char*)d_ws;
  k1_kernel<<<256, 768, 0, stream>>>(x, wqkv, w1, w2, wo, gw, ws);
  k2_kernel<<<256, 768, 0, stream>>>(ws, (u16*)d_out);
}
```

```cpp
#include <hip/hip_runtime.h>
#include <hip/hip_bf16.h>
#include <stdint.h>

typedef __attribute__((ext_vector_type(8))) short bf16x8;
typedef __attribute__((ext_vector_type(4))) short bf16x4;
typedef __attribute__((ext_vector_type(16))) float f32x16;
typedef __attribute__((ext_vector_type(4))) float f32x4;
typedef __attribute__((ext_vector_type(2))) float f32x2;
typedef __attribute__((ext_vector_type(4))) unsigned u32x4;
typedef __attribute__((ext_vector_type(2))) unsigned u32x2;
typedef __attribute__((ext_vector_type(2))) __bf16 bf16x2_t;
typedef unsigned short u16;
#define LDSP __attribute__((address_space(3)))
#define DI __device__ __forceinline__

constexpr int SEQ = 2048, DFF = 1728;
constexpr int KT = 256, VT = 1408, VS = 704, VH = 352;
constexpr int KV_VOFF = 6 * 2048;
constexpr int VSTAGE = 8 * VT;
constexpr int KV_STAGE = KV_VOFF + VSTAGE;
constexpr size_t QK_OFF = 0;
constexpr size_t CB_OFF = QK_OFF + (size_t)320 * 16384;
constexpr size_t V_OFF = CB_OFF + 2048;
constexpr size_t HF_OFF = V_OFF + (size_t)32 * 8 * VSTAGE;
constexpr size_t W2F_OFF = HF_OFF + (size_t)64 * 108 * 1024;
constexpr size_t WOF_OFF = W2F_OFF + (size_t)108 * 20 * 1024;
constexpr size_t OF_OFF = WOF_OFF + (size_t)40 * 20 * 1024;
constexpr size_t G_OFF = OF_OFF + (size_t)64 * 40 * 1024;

#define MFMA32(a, b, c) __builtin_amdgcn_mfma_f32_32x32x16_bf16((a), (b), (c), 0, 0, 0)
#define MFMA32K8(a, b, c) __builtin_amdgcn_mfma_f32_32x32x8bf16_1k((a), (b), (c), 0, 0, 0)

DI unsigned pk2(float lo, float hi) {
  f32x2 v = {lo, hi};
  bf16x2_t r = __builtin_convertvector(v, bf16x2_t);
  return __builtin_bit_cast(unsigned, r);
}
DI float bf_lo(unsigned u) { return __uint_as_float(u << 16); }
DI float bf_hi(unsigned u) { return __uint_as_float(u & 0xffff0000u); }

DI void glds16(const void* g, unsigned lds_dst) {
  unsigned sv;
  asm volatile("s_mov_b32 %0, m0\n\ts_mov_b32 m0, %2\n\ts_nop 0\n\tglobal_load_lds_dwordx4 %1, off\n\ts_mov_b32 m0, %0"
               : "=&s"(sv) : "v"(g), "s"(lds_dst) : "memory");
}
DI void glds16s(const void* sbase, unsigned voff, unsigned lds_dst) {
  unsigned sv;
  asm volatile("s_mov_b32 %0, m0\n\ts_mov_b32 m0, %3\n\ts_nop 0\n\tglobal_load_lds_dwordx4 %1, %2\n\ts_mov_b32 m0, %0"
               : "=&s"(sv) : "v"(voff), "s"(sbase), "s"(lds_dst) : "memory");
}
typedef __amdgpu_buffer_rsrc_t rsrc_t;
DI rsrc_t mk_rsrc(const void* p) { return __builtin_amdgcn_make_buffer_rsrc((void*)p, 0, 0x7fffffff, 0x00020000); }
DI bf16x8 bload(rsrc_t r, unsigned voff, unsigned soff) {
  return __builtin_bit_cast(bf16x8, __builtin_amdgcn_raw_buffer_load_b128(r, voff, soff, 0));
}
DI void st16(rsrc_t r, unsigned off, u32x4 v) { __builtin_amdgcn_raw_buffer_store_b128(v, r, off, 0, 0); }
DI bf16x4 tr_read(const LDSP char* p) {
  return __builtin_amdgcn_ds_read_tr16_b64_v4i16((LDSP bf16x4*)p);
}
#define WAITV(n) asm volatile("s_waitcnt vmcnt(" #n ")" ::: "memory")
#define BARRIER() asm volatile("s_waitcnt lgkmcnt(0)\n\ts_barrier" ::: "memory")

constexpr int K1_SLOT = 49152;

template <bool TR>
DI void k1_mainloop(f32x16 (&acc)[2][2], LDSP char* lds, const int (&aoff)[4], const int (&boff)[2]) {
  bf16x8 fa[2][2], fb[2][2];
  auto rd = [&](int buf, int kt, int ks) {
    const LDSP char* sa = lds + (kt % 3) * K1_SLOT;
    const LDSP char* sb = sa + 32768;
#pragma unroll
    for (int nt = 0; nt < 2; ++nt) {
      bf16x4 lo = tr_read(sb + boff[nt] + ks * 4096);
      bf16x4 hi = tr_read(sb + boff[nt] + ks * 4096 + 1024);
      fb[buf][nt] = __builtin_shufflevector(lo, hi, 0, 1, 2, 3, 4, 5, 6, 7);
    }
#pragma unroll
    for (int mt = 0; mt < 2; ++mt) fa[buf][mt] = *(const LDSP bf16x8*)(sa + aoff[ks] + mt * 4096);
  };
  BARRIER();
  rd(0, 0, 0);
  __builtin_amdgcn_sched_barrier(0);
#pragma unroll
  for (int kt = 0; kt < 10; ++kt) {
#pragma unroll
    for (int ks = 0; ks < 4; ++ks) {
      const int cb = ks & 1;
      if (ks < 3) rd(cb ^ 1, kt, ks + 1);
      else if (kt < 9) {
        BARRIER();
        rd(cb ^ 1, kt + 1, 0);
      }
      __builtin_amdgcn_sched_barrier(0);
#pragma unroll
      for (int mt = 0; mt < 2; ++mt)
#pragma unroll
        for (int nt = 0; nt < 2; ++nt)
          acc[mt][nt] = TR ? MFMA32(fb[cb][nt], fa[cb][mt], acc[mt][nt]) : MFMA32(fa[cb][mt], fb[cb][nt], acc[mt][nt]);
      __builtin_amdgcn_sched_barrier(0);
    }
  }
}

DI void k1_loader(int lw, int lane, unsigned lds0, const u16* xa, const u16* wb, int ldw, bool half_tile) {
  unsigned aoffs[2], boffs;
#pragma unroll
  for (int par = 0; par < 2; ++par) aoffs[par] = (lane >> 3) * 1280 + (((lane & 7) ^ ((4 * par + (lane >> 4)) & 7)) * 16);
  {
    int c = (lane & 15) ^ (((lane >> 4) & 3) << 2);
    if (half_tile) c &= 7;
    boffs = (lane >> 4) * ldw * 2 + c * 16;
  }
  auto issue = [&](int kt) {
    const unsigned sl = lds0 + (kt % 3) * K1_SLOT;
#pragma unroll
    for (int i = 0; i < 8; ++i) {
      const int pc = lw * 8 + i;
      glds16s(xa + (size_t)pc * 8 * 640 + kt * 64, aoffs[i & 1], sl + pc * 1024);
    }
#pragma unroll
    for (int i = 0; i < 4; ++i) {
      const int pc = lw * 4 + i;
      glds16s(wb + (size_t)(kt * 64 + pc * 4) * ldw, boffs, sl + 32768 + pc * 1024);
    }
  };
  issue(0);
  issue(1);
  WAITV(12);
  BARRIER();
  issue(2);
#pragma unroll
  for (int kt = 0; kt < 9; ++kt) {
    if (kt < 8) WAITV(12); else WAITV(0);
    BARRIER();
    if (kt + 3 < 10) issue(kt + 3);
  }
}

DI void k1_aux(int aux, LDSP char* lds, const u16* x, const u16* w2, const u16* wo, const u16* gw, char* ws) {
  const int tid = threadIdx.x, lane = tid & 63, wid = __builtin_amdgcn_readfirstlane(tid >> 6);
  const unsigned lds0 = (unsigned)(uintptr_t)lds;
  const rsrc_t rws = mk_rsrc(ws);
  {
    const u32x4 ones = {0x3f803f80u, 0x3f803f80u, 0x3f803f80u, 0x3f803f80u}, zeros = {0u, 0u, 0u, 0u};
    if (aux == 0 && tid < 128) st16(rws, (unsigned)CB_OFF + tid * 16, u32x4{0x00003f80u, 0u, 0x00003f80u, 0u});
    for (int i = aux * 768 + tid; i < 2048 * 8; i += 24 * 768)
      st16(rws, (unsigned)V_OFF + (i >> 3) * VT + ((i & 7) >> 1) * VH + (20 + (i & 1)) * 16, (i & 1) ? zeros : ones);
  }
  {
    const u32x4 ff = {0xffffffffu, 0xffffffffu, 0xffffffffu, 0xffffffffu};
    for (int i = aux * 768 + tid; i < 64 * 40 * 64; i += 24 * 768) st16(rws, (unsigned)OF_OFF + i * 16, ff);
  }
  constexpr int SLAB = 16 * 1344;
  for (int pc = wid; pc < 147; pc += 12) {
    const int sl = pc / 21, pp = pc - sl * 21;
    int ksg = aux + sl * 24;
    if (ksg >= 148) ksg = 147;
    const int i = pp * 64 + lane, rho = i / 84;
    int c = i - rho * 84;
    if (c > 79) c = 79;
    const bool isw2 = ksg < 108;
    const int ks = isw2 ? ksg : ksg - 108;
    int row;
    if (isw2) row = 16 * ks + rho;
    else if (ks < 32) row = 20 * ks + rho;
    else row = 20 * (4 * (ks - 32) + 2 * ((rho >> 2) & 1) + (rho >> 3)) + 16 + (rho & 3);
    glds16((isw2 ? w2 : wo) + (size_t)row * 640 + c * 8, lds0 + pc * 1024);
  }
  const int wv = aux * 12 + wid, sub = lane >> 4, l16 = lane & 15;
  u32x4 g4[5], x4[2][5];
#pragma unroll
  for (int c = 0; c < 5; ++c) g4[c] = *(const u32x4*)(gw + (c * 16 + l16) * 8);
#pragma unroll
  for (int i = 0; i < 2; ++i) {
    int row = wv * 4 + i * 1152 + sub;
    if (row >= SEQ) row = SEQ - 1;
#pragma unroll
    for (int c = 0; c < 5; ++c) x4[i][c] = *(const u32x4*)(x + (size_t)row * 640 + (c * 16 + l16) * 8);
  }
  float* gout = (float*)(ws + G_OFF);
#pragma unroll
  for (int i = 0; i < 2; ++i) {
    const int row = wv * 4 + i * 1152 + sub;
    float s = 0.f;
#pragma unroll
    for (int c = 0; c < 5; ++c)
#pragma unroll
      for (int e = 0; e < 4; ++e) { s += bf_lo(x4[i][c][e]) * bf_lo(g4[c][e]); s += bf_hi(x4[i][c][e]) * bf_hi(g4[c][e]); }
    s += __shfl_xor(s, 1); s += __shfl_xor(s, 2); s += __shfl_xor(s, 4); s += __shfl_xor(s, 8);
    if (l16 == 0 && row < SEQ) gout[row] = 1.f / (1.f + __expf(-s));
  }
  WAITV(0);
  BARRIER();
  const int toff = (4 * (lane >> 5) + ((lane & 15) >> 2)) * 1344 + ((lane >> 4) & 1) * 32 + (lane & 3) * 8;
  for (int f = wid; f < 140; f += 12) {
    const int sl = f / 20, ct = f - sl * 20;
    const int ksg = aux + sl * 24;
    if (ksg >= 148) break;
    const LDSP char* pb = lds + sl * SLAB + toff + ct * 64;
    bf16x4 lo = tr_read(pb), hi = tr_read(pb + 8 * 1344);
    bf16x8 v = __builtin_shufflevector(lo, hi, 0, 1, 2, 3, 4, 5, 6, 7);
    st16(rws, (unsigned)W2F_OFF + (ksg * 20 + ct) * 1024 + lane * 16, __builtin_bit_cast(u32x4, v));
  }
}

__global__ __launch_bounds__(768, 3) void k1_kernel(const u16* __restrict__ x, const u16* __restrict__ wqkv,
                                                 const u16* __restrict__ w1, const u16* __restrict__ w2,
                                                 const u16* __restrict__ wo, const u16* __restrict__ gw,
                                                 char* __restrict__ ws) {
  __shared__ __attribute__((aligned(16))) char lds_raw[147 * 1024];
  const int bid = blockIdx.x, xcd = bid & 7, idx = bid >> 3;
  if (idx >= 29) { k1_aux(xcd * 3 + (idx - 29), (LDSP char*)lds_raw, x, w2, wo, gw, ws); return; }
  const int ii = (xcd & 3) * 29 + idx, nti = ii >> 2, mti = (xcd >> 2) * 4 + (ii & 3);
  const int tid = threadIdx.x, lane = tid & 63, wid = __builtin_amdgcn_readfirstlane(tid >> 6);
  const int r = lane & 31, hh = lane >> 5, wm = wid >> 1, wn = wid & 1;
  const int m0 = mti * 256;
  const bool isq = nti < 15;
  const u16* W = isq ? wqkv : w1;
  const int ldw = isq ? 1920 : DFF;
  const int n0 = isq ? nti * 128 : (nti - 15) * 128;
  const bool half_tile = (nti == 28);
  LDSP char* lds = (LDSP char*)lds_raw;
  const unsigned lds0 = (unsigned)(uintptr_t)lds;

  if (wid >= 8) { k1_loader(wid - 8, lane, lds0, x + (size_t)m0 * 640, W + n0, ldw, half_tile); return; }
  int aoff[4], boff[2];
#pragma unroll
  for (int ks = 0; ks < 4; ++ks) aoff[ks] = (wm * 64 + r) * 128 + (((2 * ks + hh) ^ ((r >> 1) & 7)) * 16);
  {
    const int q = (lane & 15) >> 2, p = lane & 3, blk = (lane >> 4) & 1;
#pragma unroll
    for (int nt = 0; nt < 2; ++nt)
      boff[nt] = (8 * hh + q) * 256 + ((((wn * 2 + nt) ^ q) * 4 + 2 * blk + (p >> 1)) * 16) + 8 * (p & 1);
  }
  f32x16 acc[2][2];
#pragma unroll
  for (int mt = 0; mt < 2; ++mt)
#pragma unroll
    for (int nt = 0; nt < 2; ++nt)
#pragma unroll
      for (int e = 0; e < 16; ++e) acc[mt][nt][e] = 0.f;

  const bool isv = (nti >= 10 && nti < 15);
  const rsrc_t rws = mk_rsrc(ws);
  const int rb0 = m0 + wm * 64;
  unsigned dsto[2] = {0u, 0u};
  float sc = 1.f;
  if (isv) {
#pragma unroll
    for (int nt = 0; nt < 2; ++nt) {
      const int col = (nti - 10) * 128 + wn * 64 + nt * 32 + r, head = col / 20, d = col - head * 20;
      dsto[nt] = (unsigned)V_OFF + (head * 64 + (rb0 >> 5)) * VT + hh * VH + d * 16;
    }
  } else if (nti < 10) {
    if (nti < 5) sc = 0.22360679774997896f * 1.4426950408889634f;
    dsto[0] = (unsigned)QK_OFF + (nti * 32 + wn * 16 + hh) * 16384 + (rb0 + r) * 8;
  } else {
    dsto[0] = (unsigned)HF_OFF + ((rb0 >> 5) * 108 + (((nti - 15) * 128 + wn * 64) >> 4)) * 1024 + lane * 16;
    if (half_tile && wn == 1) dsto[0] = 0x80000000u;
  }
  const unsigned da = dsto[0], db = dsto[1];
  if (isv) {
    k1_mainloop<false>(acc, lds, aoff, boff);
    {
#pragma unroll
      for (int nt = 0; nt < 2; ++nt)
#pragma unroll
        for (int mt = 0; mt < 2; ++mt)
#pragma unroll
          for (int s = 0; s < 2; ++s) {
            u32x4 o;
#pragma unroll
            for (int e = 0; e < 4; ++e) o[e] = pk2(acc[mt][nt][8 * s + 2 * e], acc[mt][nt][8 * s + 2 * e + 1]);
            __builtin_amdgcn_raw_buffer_store_b128(o, rws, nt ? db : da, mt * VT + s * VS, 0);
          }
    }
  } else {
    k1_mainloop<true>(acc, lds, aoff, boff);
    {
      if (nti < 10) {
#pragma unroll
        for (int nt = 0; nt < 2; ++nt)
#pragma unroll
          for (int g = 0; g < 4; ++g)
#pragma unroll
            for (int mt = 0; mt < 2; ++mt) {
              u32x2 o;
              o[0] = pk2(acc[mt][nt][4 * g] * sc, acc[mt][nt][4 * g + 1] * sc);
              o[1] = pk2(acc[mt][nt][4 * g + 2] * sc, acc[mt][nt][4 * g + 3] * sc);
              __builtin_amdgcn_raw_buffer_store_b64(o, rws, da, (nt * 8 + g * 2) * 16384 + mt * 256, 0);
            }
      } else {
#pragma unroll
        for (int nt = 0; nt < 2; ++nt)
#pragma unroll
          for (int mt = 0; mt < 2; ++mt)
#pragma unroll
            for (int s = 0; s < 2; ++s) {
              u32x4 o;
#pragma unroll
              for (int e = 0; e < 4; ++e)
                o[e] = pk2(fmaxf(acc[mt][nt][8 * s + 2 * e], 0.f), fmaxf(acc[mt][nt][8 * s + 2 * e + 1], 0.f));
              __builtin_amdgcn_raw_buffer_store_b128(o, rws, da, mt * (108 * 1024) + (nt * 2 + s) * 1024, 0);
            }
      }
    }
  }
}

constexpr int K2_RING = 3 * KV_STAGE;
constexpr int PART_ROW = 656;
constexpr int PART_SZ = 32 * PART_ROW;
constexpr int K2_FLAGS = K2_RING + 4 * PART_SZ;
constexpr int K2_LDS = K2_FLAGS + 128;

DI void pack_p(const f32x16& s, bf16x8& p0, bf16x8& p1) {
  u32x4 a, b;
#pragma unroll
  for (int e = 0; e < 4; ++e) { a[e] = pk2(s[2 * e], s[2 * e + 1]); b[e] = pk2(s[8 + 2 * e], s[8 + 2 * e + 1]); }
  p0 = __builtin_bit_cast(bf16x8, a); p1 = __builtin_bit_cast(bf16x8, b);
}
DI float xhalf_max(float m) { return fmaxf(m, __shfl_xor(m, 32)); }

DI unsigned lds_ld(LDSP char* p) { return __hip_atomic_load((LDSP unsigned*)p, __ATOMIC_RELAXED, __HIP_MEMORY_SCOPE_WORKGROUP); }
DI void lds_inc(unsigned addr) {
  unsigned va, v1; unsigned long long sv;
  asm volatile("v_mov_b32 %0, %3\n\tv_mov_b32 %1, 1\n\ts_mov_b64 %2, exec\n\ts_mov_b64 exec, 1\n\t"
               "ds_add_u32 %0, %1\n\ts_mov_b64 exec, %2"
               : "=&v"(va), "=&v"(v1), "=&s"(sv) : "s"(addr) : "memory");
}
DI void lds_wait(unsigned addr, unsigned need) {
  unsigned va, vv, sv;
  asm volatile("v_mov_b32 %0, %3\n"
               "LW%=:\n\tds_read_b32 %1, %0\n\ts_waitcnt lgkmcnt(0)\n\tv_readfirstlane_b32 %2, %1\n\ts_nop 3\n\t"
               "s_cmp_ge_u32 %2, %4\n\ts_cbranch_scc1 LD%=\n\ts_sleep 1\n\ts_branch LW%=\n"
               "LD%=:"
               : "=&v"(va), "=&v"(vv), "=&s"(sv) : "s"(addr), "s"(need) : "memory", "scc");
}

DI void attn_fallback(const char* kh, const char* vh, bf16x8 q0, bf16x4 q1, int lane, f32x16& o_out) {
  const int r = lane & 31, hh = lane >> 5, rv = r < 21 ? r : 21;
  f32x16 o, zero;
#pragma unroll
  for (int e = 0; e < 16; ++e) { o[e] = 0.f; zero[e] = 0.f; }
  float m = -INFINITY;
  for (int t = 0; t < 64; ++t) {
    const char* kp = kh + (t * 32 + r) * 8;
    const bf16x4 ka = *(const bf16x4*)(kp + (2 * hh) * 16384), kb = *(const bf16x4*)(kp + (2 * hh + 1) * 16384);
    const bf16x8 kf0 = __builtin_shufflevector(ka, kb, 0, 1, 2, 3, 4, 5, 6, 7);
    const bf16x4 kf1 = *(const bf16x4*)(kp + 4 * 16384);
    const bf16x8 vf0 = *(const bf16x8*)(vh + t * VT + hh * VH + rv * 16);
    const bf16x8 vf1 = *(const bf16x8*)(vh + t * VT + VS + hh * VH + rv * 16);
    f32x16 s = MFMA32(kf0, q0, zero);
    s = MFMA32K8(kf1, q1, s);
    float tm = s[0];
#pragma unroll
    for (int e = 1; e < 16; ++e) tm = fmaxf(tm, s[e]);
    tm = xhalf_max(tm);
    const float mn = fmaxf(m, tm);
    const float alpha = __builtin_amdgcn_exp2f(m - mn);
    m = mn;
#pragma unroll
    for (int e = 0; e < 16; ++e) { o[e] *= alpha; s[e] = __builtin_amdgcn_exp2f(s[e] - mn); }
    bf16x8 p0, p1;
    pack_p(s, p0, p1);
    o = MFMA32(vf0, p0, o);
    o = MFMA32(vf1, p1, o);
  }
  o_out = o;
}

DI bool frag_ready(const bf16x8& f) {
  const u32x4 w = __builtin_bit_cast(u32x4, f);
  unsigned m = 0xffffu;
#pragma unroll
  for (int e = 0; e < 4; ++e) { const unsigned t = ~w[e]; m = min(m, min(t & 0xffffu, t >> 16)); }
  return __all(m != 0u);
}
template <int NPRE, int NL, bool POLL>
DI void oproj(rsrc_t ro, rsrc_t rw, unsigned voff, const bf16x8 (&pre)[10], LDSP char* lds, unsigned lds0, int wid,
              LDSP char* pb, float gj) {
  constexpr int NT = NPRE + NL;
  bf16x8 bfr[10][NL ? NL : 1];
#pragma unroll
  for (int k = 0; k < 10; ++k)
#pragma unroll
    for (int c = 0; c < NL; ++c) bfr[k][c] = bload(rw, voff, k * 20480 + (NPRE + c) * 1024);
  __builtin_amdgcn_sched_barrier(0);
  if (POLL) {
    lds_wait(lds0 + K2_FLAGS + 32 + 7 * 4, 8u);
    unsigned pend = 0x1fu;
    bf16x8 of[5];
    for (int spin = 0; pend != 0u && spin < (1 << 18); ++spin) {
#pragma unroll
      for (int i = 0; i < 5; ++i)
        if ((pend >> i) & 1u) of[i] = __builtin_bit_cast(bf16x8, __builtin_amdgcn_raw_buffer_load_b128(ro, voff, (wid + 8 * i) * 1024, 16));
#pragma unroll
      for (int i = 0; i < 5; ++i)
        if (((pend >> i) & 1u) && frag_ready(of[i])) {
          pend &= ~(1u << i);
          *(LDSP bf16x8*)(lds + (wid + 8 * i) * 1024 + voff) = of[i];
        }
      if (pend) __builtin_amdgcn_s_sleep(1);
    }
  }
  lds_inc(lds0 + K2_FLAGS + 72);
  lds_wait(lds0 + K2_FLAGS + 72, 12u);
  const LDSP char* ab = lds + (wid & 3) * 10240 + voff;
  f32x16 acc[NT];
#pragma unroll
  for (int c = 0; c < NT; ++c)
#pragma unroll
    for (int e = 0; e < 16; ++e) acc[c][e] = 0.f;
#pragma unroll
  for (int k = 0; k < 10; ++k) {
    const bf16x8 af = *(const LDSP bf16x8*)(ab + k * 1024);
    if (NPRE) acc[0] = MFMA32(pre[k], af, acc[0]);
#pragma unroll
    for (int c = 0; c < NL; ++c) acc[NPRE + c] = MFMA32(bfr[k][c], af, acc[NPRE + c]);
  }
#pragma unroll
  for (int c = 0; c < NT; ++c)
#pragma unroll
    for (int g = 0; g < 4; ++g) {
      f32x4 v = *(const LDSP f32x4*)(pb + c * 128 + g * 32);
#pragma unroll
      for (int e = 0; e < 4; ++e) v[e] += gj * acc[c][4 * g + e];
      *(LDSP f32x4*)(pb + c * 128 + g * 32) = v;
    }
}

__global__ __launch_bounds__(768, 3) void k2_kernel(char* __restrict__ ws, u16* __restrict__ out) {
  __shared__ __attribute__((aligned(16))) char lds_raw[K2_LDS];
  LDSP char* lds = (LDSP char*)lds_raw;
  const unsigned lds0 = (unsigned)(uintptr_t)lds;
  const int bid = blockIdx.x, xcd = bid & 7, idx = bid >> 3;
  const int tid = threadIdx.x, lane = tid & 63, wid = __builtin_amdgcn_readfirstlane(tid >> 6);
  const int r = lane & 31, hh = lane >> 5;
  if (wid < 8) {
    const int head = xcd * 4 + (idx >> 3), qt = idx & 7;
    const int rt = qt * 8 + wid;
    const char* qsrc = ws + QK_OFF + (size_t)head * 5 * 16384 + (rt * 32 + r) * 8;
    const bf16x4 qa = *(const bf16x4*)(qsrc + (2 * hh) * 16384), qb = *(const bf16x4*)(qsrc + (2 * hh + 1) * 16384);
    const bf16x8 q0 = __builtin_shufflevector(qa, qb, 0, 1, 2, 3, 4, 5, 6, 7);
    bf16x4 q1 = *(const bf16x4*)(qsrc + 4 * 16384);
    const int jrt = xcd * 8 + (idx >> 2), cg = idx & 3, kq = wid & 3, tg = wid >> 2;
    const rsrc_t rw = mk_rsrc(ws + WOF_OFF + ((size_t)kq * 10 * 20 + cg * 5 + tg * 2) * 1024);
    if (hh) q1 = bf16x4{0, 0, 0, 0};
    const int rv = r < 21 ? r : 21;
    const int koff0 = hh * 4096 + r * 8, koff1 = (4 + hh) * 2048 + r * 8, voff = KV_VOFF + hh * VH + rv * 16;
#define KLD0(P) __builtin_shufflevector(*(const LDSP bf16x4*)((P) + koff0), *(const LDSP bf16x4*)((P) + koff0 + 2048), 0, 1, 2, 3, 4, 5, 6, 7)
    f32x16 o, zero;
#pragma unroll
    for (int e = 0; e < 16; ++e) { o[e] = 0.f; zero[e] = 0.f; }
    if (tid < 32) *(LDSP unsigned*)(lds + K2_FLAGS + tid * 4) = 0u;
    BARRIER();
    bf16x8 wpre[10];
#pragma unroll
    for (int k = 0; k < 10; ++k) wpre[k] = bf16x8{0, 0, 0, 0, 0, 0, 0, 0};
    bf16x8 kf0 = KLD0(lds);
    bf16x4 kf1 = *(const LDSP bf16x4*)(lds + koff1);
    {
      f32x16 s = MFMA32(kf0, q0, zero);
      s = MFMA32K8(kf1, q1, s);
      float tm = s[0];
#pragma unroll
      for (int e = 1; e < 16; ++e) tm = fmaxf(tm, s[e]);
      tm = xhalf_max(tm);
      if (hh) q1[0] = (short)(pk2(-tm, 0.f) & 0xffffu);
    }
    f32x16 sA = MFMA32(kf0, q0, zero), sB;
    sA = MFMA32K8(kf1, q1, sA);
    kf0 = KLD0(lds + KT);
    kf1 = *(const LDSP bf16x4*)(lds + KT + koff1);
    u32x4 p0, p1 = {0u, 0u, 0u, 0u};
    bf16x8 vf0, vf1 = {0, 0, 0, 0, 0, 0, 0, 0};
#define SB() __builtin_amdgcn_sched_barrier(0)
#define EX(S, i) S[i] = __builtin_amdgcn_exp2f(S[i])
#define TILE(SC, SN, VP, KP)                                                                   \
  do {                                                                                         \
    EX(SC, 0); EX(SC, 1); SB();                                                                \
    o = MFMA32(vf1, __builtin_bit_cast(bf16x8, p1), o); SB();                                  \
    vf0 = *(const LDSP bf16x8*)((VP)); vf1 = *(const LDSP bf16x8*)((VP) + VS);                \
    EX(SC, 2); EX(SC, 3); p0[0] = pk2(SC[0], SC[1]); p0[1] = pk2(SC[2], SC[3]);                \
    EX(SC, 4); EX(SC, 5); SB();                                                                \
    SN = MFMA32(kf0, q0, zero); SB();                                                          \
    EX(SC, 6); EX(SC, 7); p0[2] = pk2(SC[4], SC[5]); p0[3] = pk2(SC[6], SC[7]);                \
    EX(SC, 8); EX(SC, 9); SB();                                                                \
    SN = MFMA32K8(kf1, q1, SN); SB();                                                          \
    kf0 = KLD0(KP); kf1 = *(const LDSP bf16x4*)((KP) + koff1);      \
    EX(SC, 10); EX(SC, 11); p1[0] = pk2(SC[8], SC[9]); p1[1] = pk2(SC[10], SC[11]);            \
    EX(SC, 12); EX(SC, 13); SB();                                                              \
    o = MFMA32(vf0, __builtin_bit_cast(bf16x8, p0), o); SB();                                  \
    EX(SC, 14); EX(SC, 15); p1[2] = pk2(SC[12], SC[13]); p1[3] = pk2(SC[14], SC[15]); SB();    \
  } while (0)
    int slot = 0;
    for (int stage = 0; stage < 8; ++stage) {
      const int nslot = slot == 2 ? 0 : slot + 1;
      const LDSP char* cb = lds + slot * KV_STAGE;
      const LDSP char* nb = lds + nslot * KV_STAGE;
      const LDSP char* vb = cb + voff;
      if (stage == 2) {
#pragma unroll
        for (int k = 0; k < 10; ++k) wpre[k] = bload(rw, lane * 16, k * 20480);
        SB();
      }
      TILE(sA, sB, vb + 0 * VT, cb + 2 * KT);
      TILE(sB, sA, vb + 1 * VT, cb + 3 * KT);
      TILE(sA, sB, vb + 2 * VT, cb + 4 * KT);
      TILE(sB, sA, vb + 3 * VT, cb + 5 * KT);
      TILE(sA, sB, vb + 4 * VT, cb + 6 * KT);
      const unsigned full = lds_ld(lds + K2_FLAGS + ((stage + 1) & 7) * 4);
      SB();
      TILE(sB, sA, vb + 5 * VT, cb + 7 * KT);
      if (stage < 7 && (unsigned)__builtin_amdgcn_readfirstlane(full) < 4u) lds_wait(lds0 + K2_FLAGS + (stage + 1) * 4, 4u);
      asm volatile("" ::: "memory");
      TILE(sA, sB, vb + 6 * VT, nb + 0 * KT);
      TILE(sB, sA, vb + 7 * VT, nb + 1 * KT);
      lds_inc(lds0 + K2_FLAGS + 32 + stage * 4);
      slot = nslot;
    }
    o = MFMA32(vf1, __builtin_bit_cast(bf16x8, p1), o);
    {
      float l = __shfl(o[8], 32 + r);
      float amax = 0.f;
#pragma unroll
      for (int e = 0; e < 12; ++e) amax = fmaxf(amax, fabsf(o[e]));
      bool bad = !(l <= 1.0e36f) || !(l >= 8.7e-19f) || !(amax <= 3.0e38f);
#pragma unroll
      for (int e = 0; e < 12; ++e) bad = bad || (o[e] != o[e]);
      if (__any(bad)) { if (hh) q1[0] = 0; attn_fallback(ws + QK_OFF + (size_t)(160 + head * 5) * 16384, ws + V_OFF + (size_t)head * 64 * VT, q0, q1, lane, o); }
    }
    const float l = __shfl(o[8], 32 + r);
    const float inv = 1.f / l;
    const rsrc_t rws = mk_rsrc(ws);
    const unsigned odst = (unsigned)OF_OFF + rt * 40 * 1024;
#pragma unroll
    for (int e = 0; e < 12; ++e) {
      const float v = o[e] * inv;
      o[e] = (v != v) ? __uint_as_float(0x7fc00000u) : v;
    }
    u32x4 om;
#pragma unroll
    for (int e = 0; e < 4; ++e) om[e] = pk2(o[2 * e], o[2 * e + 1]);
    __builtin_amdgcn_raw_buffer_store_b128(om, rws, odst + head * 1024 + lane * 16, 0, 16);
    if (hh == 0) {
      u32x2 orr;
      orr[0] = pk2(o[8], o[9]);
      orr[1] = pk2(o[10], o[11]);
      __builtin_amdgcn_raw_buffer_store_b64(orr, rws, odst + (32 + (head >> 2)) * 1024 + (((head >> 1) & 1) * 32 + r) * 16 + (head & 1) * 8, 0, 16);
    }
    const rsrc_t ro = mk_rsrc(ws + OF_OFF + (size_t)jrt * 40 * 1024);
    LDSP char* pb = lds + K2_RING + kq * PART_SZ + r * PART_ROW + hh * 16 + tg * 2 * 128;
    const float gj = *(const float*)(ws + G_OFF + (size_t)(jrt * 32 + r) * 4);
    if (tg == 0) oproj<1, 1, true>(ro, rw, lane * 16, wpre, lds, lds0, wid, pb, gj);
    else oproj<1, 0, true>(ro, rw, lane * 16, wpre, lds, lds0, wid, pb, gj);
  } else {
    const int kq = wid - 8;
    const int rt = xcd * 8 + (idx >> 2), cg = idx & 3;
    const rsrc_t ra = mk_rsrc(ws + HF_OFF + ((size_t)rt * 108 + kq * 27) * 1024);
    const rsrc_t rb = mk_rsrc(ws + W2F_OFF + ((size_t)kq * 27 * 20 + cg * 5) * 1024);
    const unsigned voff = lane * 16;
    const int head = xcd * 4 + (idx >> 3);
    const char* pbase[6];
    unsigned pstep[6], pdst[6];
#pragma unroll
    for (int i = 0; i < 6; ++i) {
      const int pc = min(kq + 4 * i, 22);
      pdst[i] = lds0 + pc * 1024;
      if (pc < 10) { pbase[i] = ws + QK_OFF + (size_t)(160 + head * 5 + (pc >> 1)) * 16384 + (pc & 1) * 1024; pstep[i] = 2048; }
      else if (pc < 12) { pbase[i] = ws + CB_OFF + (pc - 10) * 1024; pstep[i] = 0; }
      else { pbase[i] = ws + V_OFF + (size_t)head * 8 * VSTAGE + (pc - 12) * 1024; pstep[i] = VSTAGE; }
    }
    auto dma = [&](int stage) {
#pragma unroll
      for (int i = 0; i < 6; ++i) glds16s(pbase[i] + (size_t)stage * pstep[i], voff, pdst[i] + (stage % 3) * KV_STAGE);
    };
    dma(0);
    dma(1);
    f32x16 acc[5];
#pragma unroll
    for (int c = 0; c < 5; ++c)
#pragma unroll
      for (int e = 0; e < 16; ++e) acc[c][e] = 0.f;
    constexpr int PD = 3;
    bf16x8 af[PD], bfr[PD][5];
#pragma unroll
    for (int j = 0; j < PD; ++j) {
      af[j] = bload(ra, voff, j * 1024);
#pragma unroll
      for (int c = 0; c < 5; ++c) bfr[j][c] = bload(rb, voff, j * 20480 + c * 1024);
    }
    __builtin_amdgcn_sched_barrier(0);
    WAITV(24);
    BARRIER();
    __builtin_amdgcn_sched_barrier(0);
    constexpr int KPS = 3;
    auto kstep = [&](int k) {
      const int j = k % PD;
#pragma unroll
      for (int c = 0; c < 5; ++c) acc[c] = MFMA32(bfr[j][c], af[j], acc[c]);
      __builtin_amdgcn_sched_barrier(0);
      if (k + PD < 27) {
        af[j] = bload(ra, voff, (k + PD) * 1024);
#pragma unroll
        for (int c = 0; c < 5; ++c) bfr[j][c] = bload(rb, voff, (k + PD) * 20480 + c * 1024);
      }
      __builtin_amdgcn_sched_barrier(0);
    };
#pragma unroll
    for (int stage = 0; stage < 7; ++stage) {
#pragma unroll
      for (int i = 0; i < KPS; ++i) {
        kstep(stage * KPS + i);
        if (i == 1) {
          WAITV(12);
          lds_inc(lds0 + K2_FLAGS + (stage + 1) * 4);
          __builtin_amdgcn_sched_barrier(0);
        }
      }
      if (stage + 2 < 8) {
        if (stage >= 1) lds_wait(lds0 + K2_FLAGS + 32 + (stage - 1) * 4, 8u);
        dma(stage + 2);
      }
      __builtin_amdgcn_sched_barrier(0);
    }
    lds_wait(lds0 + K2_FLAGS + 32 + 7 * 4, 4u);
#pragma unroll
    for (int k = 7 * KPS; k < 27; ++k) kstep(k);
    {
      unsigned vy = voff;
      asm volatile("" : "+v"(vy));
      const int l2 = vy >> 4;
      const float gj = *(const float*)(ws + G_OFF + (size_t)(rt * 32 + (l2 & 31)) * 4);
      const float h = 1.f - gj;
      LDSP char* pb = lds + K2_RING + kq * PART_SZ + (l2 & 31) * PART_ROW + (l2 >> 5) * 16;
#pragma unroll
      for (int c = 0; c < 5; ++c)
#pragma unroll
        for (int g = 0; g < 4; ++g) {
          f32x4 v = {h * acc[c][4 * g], h * acc[c][4 * g + 1], h * acc[c][4 * g + 2], h * acc[c][4 * g + 3]};
          *(LDSP f32x4*)(pb + c * 128 + g * 32) = v;
        }
      const rsrc_t ro = mk_rsrc(ws + OF_OFF + (size_t)rt * 40 * 1024);
      const rsrc_t rw = mk_rsrc(ws + WOF_OFF + ((size_t)kq * 10 * 20 + cg * 5 + 3) * 1024);
      bf16x8 none[10];
      oproj<0, 2, false>(ro, rw, vy, none, lds, lds0, wid, pb + 3 * 128, gj);
    }
  }
  BARRIER();
  {
    const int jrt = xcd * 8 + (idx >> 2), cg = idx & 3;
    if (tid < 640) {
      const int row = tid / 20, c8 = tid - row * 20;
      const LDSP char* p = lds + K2_RING + row * PART_ROW + c8 * 32;
      f32x4 s0 = *(const LDSP f32x4*)(p), s1 = *(const LDSP f32x4*)(p + 16);
#pragma unroll
      for (int w = 1; w < 4; ++w) {
        s0 += *(const LDSP f32x4*)(p + w * PART_SZ);
        s1 += *(const LDSP f32x4*)(p + w * PART_SZ + 16);
      }
      const u32x4 o = {pk2(s0[0], s0[1]), pk2(s0[2], s0[3]), pk2(s1[0], s1[1]), pk2(s1[2], s1[3])};
      *(u32x4*)(out + (size_t)(jrt * 32 + row) * 640 + cg * 160 + c8 * 8) = o;
    }
  }
}

extern "C" void kernel_launch(void* const* d_in, const int* in_sizes, int n_in, void* d_out, int out_size, void* d_ws,
                              size_t ws_size, hipStream_t stream) {
  const u16* x = (const u16*)d_in[0];
  const u16* wqkv = (const u16*)d_in[1];
  const u16* wo = (const u16*)d_in[2];
  const u16* w1 = (const u16*)d_in[4];
  const u16* w2 = (const u16*)d_in[5];
  const u16* gw = (const u16*)d_in[6];
  char* ws = (char*)d_ws;
  k1_kernel<<<256, 768, 0, stream>>>(x, wqkv, w1, w2, wo, gw, ws);
  k2_kernel<<<256, 768, 0, stream>>>(ws, (u16*)d_out);
}
```
